```python
import torch

def _softmax(x, axis=-1):
    return torch.softmax(x, dim=axis)
def _gelu(x):
    return torch.nn.functional.gelu(x, approximate="tanh")
def _silu(x):
    return torch.nn.functional.silu(x)
def _sigmoid(x):
    return torch.sigmoid(x)
def _layernorm(x, axis=-1, eps=1e-5):
    m = x.mean(dim=axis, keepdim=True)
    v = x.var(dim=axis, keepdim=True, unbiased=False)
    return (x - m) / torch.sqrt(v + eps)
def _scatter_add(tgt, idx, src, axis=0):
    return tgt.clone().index_add_(axis, idx, src)
def _segment_sum(src, seg, n):
    out = torch.zeros((n,) + src.shape[1:], dtype=src.dtype)
    return out.index_add_(0, seg, src)
def _pad(x, pw):
    flat = tuple(v for pair in reversed(pw) for v in pair)
    return torch.nn.functional.pad(x, flat)

kernel_name = 'gated_mix__mha_block__sliding_block__prompt'

def setup_inputs():

    _w = (lambda a: (lambda t: t.to(torch.bfloat16) if t.is_floating_point() else t)(torch.as_tensor(a)))
    def _A_setup():
        B, S, D, H = 1, 2048, 640, 32
        return {
            "x": _w((lambda *s: torch.randn(*s, dtype=torch.bfloat16) * 0.05)(B, S, D)),
            "wqkv": _w((lambda *s: torch.randn(*s, dtype=torch.bfloat16) * 3.2)(D, 3 * D)),
            "wo": _w((lambda *s: torch.randn(*s, dtype=torch.bfloat16) * 0.1)(D, D)),
        }
    _A_in = _A_setup()

    def _B_setup():
        B, S, D = 1, 2048, 640
        return {
            "x": _w((lambda *s: torch.randn(*s, dtype=torch.bfloat16) * 0.05)(B, S, D)),
            "wqkv": _w((lambda *s: torch.randn(*s, dtype=torch.bfloat16) * 3.2)(D, 3 * D)),
            "wo": _w((lambda *s: torch.randn(*s, dtype=torch.bfloat16) * 0.1)(D, D)),
        }
    _B_in = _B_setup()

    return {
        "A_x": _A_in["x"],
        "A_wqkv": _A_in["wqkv"],
        "A_wo": _A_in["wo"],
        "B_x": _B_in["x"],
        "B_wqkv": _B_in["wqkv"],
        "B_wo": _B_in["wo"],
        "gate_w": _w((lambda *s: torch.randn(*s, dtype=torch.bfloat16) * 0.05)(640) * 16.0),
    }

def reference(A_x, A_wqkv, A_wo, B_x, B_wqkv, B_wo, gate_w):
    def _A_ref(x, wqkv, wo):
        B, S, D = x.shape
        H = 32
        Dh = D // H
        qkv = (lambda x, *d: x.permute(*d))((x @ wqkv).reshape(B, S, 3, H, Dh), 2, 0, 3, 1, 4)
        q, k, v = qkv[0], qkv[1], qkv[2]
        scores = torch.einsum("bhqd,bhkd->bhqk", q, k) * (Dh ** -0.5)
        p = _softmax(scores, axis=-1)
        o = torch.einsum("bhqk,bhkd->bhqd", p, v)
        return (lambda x, *d: x.permute(*d))(o, 0, 2, 1, 3).reshape(B, S, D) @ wo

    def _B_ref(x, wqkv, wo):
        B, S, D = x.shape
        H = 32; Dh = D // H; W = 256
        qkv = (lambda x, *d: x.permute(*d))((x @ wqkv).reshape(B, S, 3, H, Dh), 2, 0, 3, 1, 4)
        q, k, v = qkv[0], qkv[1], qkv[2]
        scores = torch.einsum("bhqd,bhkd->bhqk", q, k) * (Dh ** -0.5)
        idx = torch.arange(S)
        mask = (idx[:, None] - idx[None, :] >= 0) & (idx[:, None] - idx[None, :] < W)
        scores = torch.where(mask[None, None], scores, -1e9)
        p = _softmax(scores, axis=-1)
        o = torch.einsum("bhqk,bhkd->bhqd", p, v)
        return (lambda x, *d: x.permute(*d))(o, 0, 2, 1, 3).reshape(B, S, D) @ wo

    x = A_x
    g = _sigmoid((x * gate_w).sum(-1, keepdims=True))
    a = _A_ref(x, A_wqkv, A_wo)
    b = _B_ref(x, B_wqkv, B_wo)
    return g * a + (1 - g) * b
```

```cpp
#include <hip/hip_runtime.h>
#include <hip/hip_bf16.h>
#include <stdint.h>

typedef __attribute__((ext_vector_type(8))) short bf16x8;
typedef __attribute__((ext_vector_type(4))) short bf16x4;
typedef __attribute__((ext_vector_type(2))) __bf16 bf16v2;
typedef __attribute__((ext_vector_type(2))) float f32x2;
typedef __attribute__((ext_vector_type(16))) float f32x16;
typedef __attribute__((ext_vector_type(4))) float f32x4;
typedef __attribute__((ext_vector_type(4))) unsigned u32x4;
typedef __attribute__((ext_vector_type(2))) unsigned u32x2;
typedef __attribute__((address_space(3))) const char* lds_cptr;
typedef short v4i16_t __attribute__((ext_vector_type(4)));

#define DI __device__ __forceinline__
#define MFMA32(a, b, c) __builtin_amdgcn_mfma_f32_32x32x16_bf16((a), (b), (c), 0, 0, 0)
#define MFMA32K8(a, b, c) __builtin_amdgcn_mfma_f32_32x32x8bf16_1k((a), (b), (c), 0, 0, 0)
#define MFMA16(a, b, c) __builtin_amdgcn_mfma_f32_16x16x32_bf16((a), (b), (c), 0, 0, 0)

constexpr int SEQ = 2048, DM = 640, NH = 32, DH = 20, N3 = 1920;
constexpr int CHUNK = 22 * 1024;
constexpr int OFF_K2 = 8192, OFF_V = 10240;
constexpr size_t WS_KV = 0;
constexpr size_t WS_KV_BYTES = (size_t)2 * 32 * 8 * CHUNK;
constexpr int QIMG = 81920;
constexpr size_t WS_Q = WS_KV + WS_KV_BYTES;
constexpr size_t WS_Q_BYTES = (size_t)2 * 32 * QIMG;
constexpr size_t WS_O = WS_Q + WS_Q_BYTES;
constexpr size_t WS_O_BYTES = (size_t)2 * 128 * 20 * 1024;
constexpr size_t WS_WO = WS_O + WS_O_BYTES;
constexpr size_t WS_WO_BYTES = (size_t)2 * 40 * 20 * 1024;
constexpr size_t WS_G = WS_WO + WS_WO_BYTES;

constexpr float QSCALE = 0.22360679774997896f * 1.4426950408889634f;
constexpr float MBIAS = 64.0f;

DI unsigned pack2(float lo, float hi) {
    f32x2 v = {lo, hi};
    bf16v2 b = __builtin_convertvector(v, bf16v2);
    return __builtin_bit_cast(unsigned, b);
}
DI float bf2f(unsigned short b) { return __uint_as_float(((unsigned)b) << 16); }

DI void glds16(const void* g, unsigned lds_base) {
    unsigned sv;
    asm volatile("s_mov_b32 %0, m0\n\ts_mov_b32 m0, %2\n\ts_nop 0\n\tglobal_load_lds_dwordx4 %1, off\n\ts_mov_b32 m0, %0"
                 : "=&s"(sv) : "v"(g), "s"(lds_base) : "memory");
}
#define WAIT_BAR(N) asm volatile("s_waitcnt vmcnt(" #N ") lgkmcnt(0)\n\ts_barrier" ::: "memory")

DI bf16x8 lds_b128(lds_cptr p) { return *(const __attribute__((address_space(3))) bf16x8*)p; }
DI bf16x4 lds_b64(lds_cptr p) { return *(const __attribute__((address_space(3))) bf16x4*)p; }
DI bf16x4 lds_tr(lds_cptr p) {
    return __builtin_bit_cast(bf16x4, __builtin_amdgcn_ds_read_tr16_b64_v4i16((__attribute__((address_space(3))) v4i16_t*)p));
}

constexpr int G_STAGE = 48 * 1024, G_BOFF = 16 * 1024, G_LDS = 3 * G_STAGE;

template <bool ISV>
DI void qkv_mainloop(f32x16 (&acc)[2][2], char* lds, unsigned lds0, const char* asrc, const char* bsrc, int w, int lane) {
    const int r32 = lane & 31, h = lane >> 5;
    const int wm = w & 1, wn = w >> 1;
    const int arow = wm * 64 + r32;
    const int asw = (arow >> 1) & 7;
    int aoff[4];
#pragma unroll
    for (int kk = 0; kk < 4; ++kk) aoff[kk] = arow * 128 + (((2 * kk + h) ^ asw) << 4);
    const int g = lane >> 4, q = (lane & 15) >> 2, p = lane & 3;
    int boff[2];
#pragma unroll
    for (int nt = 0; nt < 2; ++nt)
        boff[nt] = G_BOFF + (8 * (g >> 1) + q) * 512 + (((2 * wn + nt) ^ q) << 6) + (16 * (g & 1) + 4 * p) * 2;

    auto issue = [&](int it, int st) {
        const unsigned base = lds0 + st * G_STAGE;
        const char* a = asrc + it * 128;
        glds16(a, (unsigned)__builtin_amdgcn_readfirstlane(base + w * 1024));
        glds16(a + 64 * DM * 2, (unsigned)__builtin_amdgcn_readfirstlane(base + (w + 8) * 1024));
        const char* b = bsrc + (size_t)it * 64 * N3 * 2;
#pragma unroll
        for (int ii = 0; ii < 4; ++ii)
            glds16(b + (size_t)ii * 16 * N3 * 2, (unsigned)__builtin_amdgcn_readfirstlane(base + G_BOFF + (w + 8 * ii) * 1024));
    };
    issue(0, 0);
    issue(1, 1);
    int st = 0;
    for (int it = 0; it < 10; ++it) {
        if (it < 9) WAIT_BAR(6); else WAIT_BAR(0);
        if (it + 2 < 10) { int s2 = st + 2; if (s2 >= 3) s2 -= 3; issue(it + 2, s2); }
        lds_cptr sb = (lds_cptr)lds + st * G_STAGE;
#pragma unroll
        for (int kk = 0; kk < 4; ++kk) {
            bf16x8 af[2], bfr[2];
#pragma unroll
            for (int mt = 0; mt < 2; ++mt) af[mt] = lds_b128(sb + aoff[kk] + mt * 4096);
#pragma unroll
            for (int nt = 0; nt < 2; ++nt) {
                bf16x4 lo = lds_tr(sb + boff[nt] + kk * 8192);
                bf16x4 hi = lds_tr(sb + boff[nt] + kk * 8192 + 2048);
                bfr[nt] = __builtin_shufflevector(lo, hi, 0, 1, 2, 3, 4, 5, 6, 7);
            }
#pragma unroll
            for (int nt = 0; nt < 2; ++nt)
#pragma unroll
                for (int mt = 0; mt < 2; ++mt) {
                    if (ISV) acc[nt][mt] = MFMA32(af[mt], bfr[nt], acc[nt][mt]);
                    else acc[nt][mt] = MFMA32(bfr[nt], af[mt], acc[nt][mt]);
                }
        }
        st = (st == 2) ? 0 : st + 1;
    }
}

__global__ __launch_bounds__(512, 2) void qkv_kernel(const __hip_bfloat16* __restrict__ x, const __hip_bfloat16* __restrict__ wA,
                                                     const __hip_bfloat16* __restrict__ wB, const __hip_bfloat16* __restrict__ woA,
                                                     const __hip_bfloat16* __restrict__ woB, const __hip_bfloat16* __restrict__ gw,
                                                     char* __restrict__ ws) {
    extern __shared__ __attribute__((aligned(16))) char lds[];
    const int tid = threadIdx.x, lane = tid & 63;
    const int w = __builtin_amdgcn_readfirstlane(tid >> 6);
    const int bid = blockIdx.x;
    if (bid >= 240) {
        const int sb = bid - 240;
        const unsigned short* xs = (const unsigned short*)x;
        const unsigned short* gs = (const unsigned short*)gw;
        float* gout = (float*)(ws + WS_G);
        for (int rr = 0; rr < 16; ++rr) {
            const int row = sb * 128 + w * 16 + rr;
            float s = 0.f;
#pragma unroll
            for (int i = 0; i < 5; ++i) {
                const unsigned xv = *(const unsigned*)(xs + (size_t)row * DM + lane * 10 + 2 * i);
                const unsigned gv = *(const unsigned*)(gs + lane * 10 + 2 * i);
                s += __uint_as_float(xv << 16) * __uint_as_float(gv << 16);
                s += __uint_as_float(xv & 0xffff0000u) * __uint_as_float(gv & 0xffff0000u);
            }
#pragma unroll
            for (int o = 32; o > 0; o >>= 1) s += __shfl_xor(s, o);
            if (lane == 0) gout[row] = 1.0f / (1.0f + __expf(-s));
        }
        const int gwv = sb * 8 + w;
        for (int f = gwv; f < 1600; f += 128) {
            const int br = f / 800, rem = f % 800, nt = rem / 20, ks = rem % 20;
            const unsigned short* src = (const unsigned short*)(br ? woB : woA) + (size_t)(ks * 32 + 8 * (lane >> 4)) * DM + nt * 16 + (lane & 15);
            unsigned v[4];
#pragma unroll
            for (int j = 0; j < 4; ++j) v[j] = (unsigned)src[(2 * j) * DM] | ((unsigned)src[(2 * j + 1) * DM] << 16);
            *(u32x4*)(ws + WS_WO + (size_t)f * 1024 + lane * 16) = (u32x4){v[0], v[1], v[2], v[3]};
        }
        return;
    }
    const int t = (bid & 7) * 30 + (bid >> 3);
    const int n_tile = t >> 4, m_tile = t & 15;
    const int m0 = m_tile * 128, n0 = n_tile * 256;
    const unsigned lds0 = (unsigned)(uintptr_t)lds;
    const char* asrc;
    {
        const int row = 8 * w + (lane >> 3);
        const int ch = (lane & 7) ^ ((row >> 1) & 7);
        asrc = (const char*)(x + (size_t)(m0 + row) * DM + ch * 8);
    }
    const char* bsrc;
    {
        const int k = 2 * w + (lane >> 5);
        const int s16 = lane & 31;
        const int c64 = (s16 >> 2) ^ (k & 3);
        const int col = n0 + c64 * 32 + (s16 & 3) * 8;
        const __hip_bfloat16* wp = (col < N3) ? (wA + col) : (wB + (col - N3));
        bsrc = (const char*)(wp + (size_t)k * N3);
    }
    const int wm = w & 1, wn = w >> 1;
    const int ncol = n0 + wn * 64;
    const int br = ncol >= N3 ? 1 : 0;
    const int nb = ncol - br * N3;
    const int part = nb / DM;
    const int np = nb - part * DM;
    f32x16 acc[2][2];
#pragma unroll
    for (int i = 0; i < 2; ++i)
#pragma unroll
        for (int j = 0; j < 2; ++j) acc[i][j] = (f32x16){};
    const int r32 = lane & 31, h = lane >> 5;
    if (part == 2) {
        qkv_mainloop<true>(acc, lds, lds0, asrc, bsrc, w, lane);
#pragma unroll
        for (int nt = 0; nt < 2; ++nt) {
            const int nn = np + nt * 32 + r32;
            const int head = nn / DH, d = nn - head * DH;
#pragma unroll
            for (int mt = 0; mt < 2; ++mt) {
                const int tok0 = m0 + wm * 64 + mt * 32;
                char* base = ws + WS_KV + (size_t)((br * 32 + head) * 8 + (tok0 >> 8)) * CHUNK + OFF_V + ((tok0 >> 5) & 7) * 1536 + h * 384;
#pragma unroll
                for (int s = 0; s < 2; ++s) {
                    const f32x16& a = acc[nt][mt];
                    u32x4 v = {pack2(a[8 * s], a[8 * s + 1]), pack2(a[8 * s + 2], a[8 * s + 3]), pack2(a[8 * s + 4], a[8 * s + 5]),
                               pack2(a[8 * s + 6], a[8 * s + 7])};
                    *(u32x4*)(base + s * 768 + d * 16) = v;
                    if (d == 0) *(u32x4*)(base + s * 768 + 320) = (u32x4){0x3f803f80u, 0x3f803f80u, 0x3f803f80u, 0x3f803f80u};
                }
            }
        }
    } else {
        qkv_mainloop<false>(acc, lds, lds0, asrc, bsrc, w, lane);
        const float sc = part == 0 ? QSCALE : 1.0f;
#pragma unroll
        for (int nt = 0; nt < 2; ++nt) {
#pragma unroll
            for (int rg = 0; rg < 4; ++rg) {
                const int nn = np + nt * 32 + 8 * rg + 4 * h;
                const int head = nn / DH, gq = (nn - head * DH) >> 2;
#pragma unroll
                for (int mt = 0; mt < 2; ++mt) {
                    const int tok = m0 + wm * 64 + mt * 32 + r32;
                    const f32x16& a = acc[nt][mt];
                    u32x2 v = {pack2(a[4 * rg] * sc, a[4 * rg + 1] * sc), pack2(a[4 * rg + 2] * sc, a[4 * rg + 3] * sc)};
                    char* dst;
                    if (part == 0) {
                        char* base = ws + WS_Q + (size_t)(br * 32 + head) * QIMG;
                        dst = gq < 4 ? base + (gq >> 1) * 32768 + tok * 16 + (gq & 1) * 8 : base + 65536 + tok * 8;
                    } else {
                        char* base = ws + WS_KV + (size_t)((br * 32 + head) * 8 + (tok >> 8)) * CHUNK;
                        const int tk = tok & 255;
                        dst = gq < 4 ? base + (gq >> 1) * 4096 + tk * 16 + (gq & 1) * 8 : base + OFF_K2 + tk * 8;
                    }
                    *(u32x2*)dst = v;
                }
            }
        }
    }
}

constexpr int A_NSLOT = 6, A_LDS = A_NSLOT * CHUNK;

DI float half_swap_max(float m) {
    auto rr = __builtin_amdgcn_permlane32_swap(__float_as_uint(m), __float_as_uint(m), false, false);
    return __builtin_fmaxf(__uint_as_float(rr[0]), __uint_as_float(rr[1]));
}
DI float rowmax16(const f32x16& s) {
    float a = __builtin_fmaxf(__builtin_fmaxf(s[0], s[1]), s[2]);
#pragma unroll
    for (int r = 3; r < 15; r += 2) a = __builtin_fmaxf(__builtin_fmaxf(a, s[r]), s[r + 1]);
    a = __builtin_fmaxf(a, s[15]);
    return half_swap_max(a);
}
DI void softmax_pv(const f32x16& s, const bf16x8& v0, const bf16x8& v1, f32x16& o) {
    float p[16];
#pragma unroll
    for (int r = 0; r < 16; ++r) p[r] = __builtin_amdgcn_exp2f(s[r]);
    u32x4 p0 = {pack2(p[0], p[1]), pack2(p[2], p[3]), pack2(p[4], p[5]), pack2(p[6], p[7])};
    u32x4 p1 = {pack2(p[8], p[9]), pack2(p[10], p[11]), pack2(p[12], p[13]), pack2(p[14], p[15])};
    o = MFMA32(v0, __builtin_bit_cast(bf16x8, p0), o);
    o = MFMA32(v1, __builtin_bit_cast(bf16x8, p1), o);
}
DI int crow(int r, int hi) { return (r & 3) + 8 * (r >> 2) + 4 * hi; }

DI void attn_slow(f32x16& o, const char* kv, const bf16x8 q1, const bf16x4 q2, int ubeg, int uend, int mode,
                                       int qpos, int lane) {
    const int r32 = lane & 31, h = lane >> 5;
    const int vrow = r32 <= 20 ? r32 : 20;
    float m = -INFINITY;
    o = (f32x16){};
    for (int i = uend; i >= ubeg; --i) {
        const char* cb = kv + (size_t)(i >> 3) * CHUNK;
        const int t = i & 7;
        bf16x8 k1 = *(const bf16x8*)(cb + h * 4096 + (t * 32 + r32) * 16);
        bf16x4 k2 = *(const bf16x4*)(cb + OFF_K2 + (t * 32 + r32) * 8);
        bf16x8 v0 = *(const bf16x8*)(cb + OFF_V + t * 1536 + h * 384 + vrow * 16);
        bf16x8 v1 = *(const bf16x8*)(cb + OFF_V + t * 1536 + 768 + h * 384 + vrow * 16);
        f32x16 s = (f32x16){};
        s = MFMA32(k1, q1, s);
        s = MFMA32K8(k2, q2, s);
        if (mode) {
#pragma unroll
            for (int r = 0; r < 16; ++r) {
                const int dlt = qpos - (i * 32 + crow(r, h));
                if (dlt < 0 || dlt >= 256) s[r] = -INFINITY;
            }
        }
        const float mn = __builtin_fmaxf(m, rowmax16(s));
        const float alpha = __builtin_amdgcn_exp2f(m - mn);
        m = mn;
#pragma unroll
        for (int r = 0; r < 16; ++r) { o[r] *= alpha; s[r] -= mn; }
        softmax_pv(s, v0, v1, o);
    }
}

DI void store_o(const f32x16& o, float l, char* oimg, int head, int qrow, int h) {
    const float rl = 1.0f / l;
    char* base = oimg + (size_t)(qrow >> 4) * 20 * 1024 + (qrow & 15) * 16;
#pragma unroll
    for (int rg = 0; rg < 3; ++rg) {
        const int d0 = 8 * rg + 4 * h;
        if (d0 < DH) {
            const int k = head * DH + d0;
            u32x2 v = {pack2(o[4 * rg] * rl, o[4 * rg + 1] * rl), pack2(o[4 * rg + 2] * rl, o[4 * rg + 3] * rl)};
            *(u32x2*)(base + (k >> 5) * 1024 + ((k >> 3) & 3) * 256 + ((k >> 2) & 1) * 8) = v;
        }
    }
}
DI float get_l(const f32x16& o) {
    auto rr = __builtin_amdgcn_permlane32_swap(__float_as_uint(o[8]), __float_as_uint(o[8]), false, false);
    return __uint_as_float(rr[1]);
}

__global__ __launch_bounds__(512, 2) void attn_kernel(char* __restrict__ ws) {
    extern __shared__ __attribute__((aligned(16))) char lds[];
    const int tid = threadIdx.x, lane = tid & 63, r32 = lane & 31, h = lane >> 5;
    const int w = __builtin_amdgcn_readfirstlane(tid >> 6);
    const int bid = blockIdx.x;
    const int head = (bid & 7) * 4 + (bid >> 6), j = (bid >> 3) & 7;
    const unsigned lds0 = (unsigned)(uintptr_t)lds;
    const char* kvA = ws + WS_KV + (size_t)(head * 8) * CHUNK;
    const char* kvB = ws + WS_KV + (size_t)((32 + head) * 8) * CHUNK;
    const int qrow = j * 256 + w * 32 + r32;

    auto issue = [&](int c) {
        const char* src = c < 8 ? kvA + (size_t)((c + j) & 7) * CHUNK : kvB + (size_t)(c == 8 ? j : (j > 0 ? j - 1 : 0)) * CHUNK;
        const unsigned dst = lds0 + (c % A_NSLOT) * CHUNK;
        src += w * 1024 + lane * 16;
        glds16(src, (unsigned)__builtin_amdgcn_readfirstlane(dst + w * 1024));
        glds16(src + 8192, (unsigned)__builtin_amdgcn_readfirstlane(dst + (w + 8) * 1024));
        if (w < 6) glds16(src + 16384, (unsigned)__builtin_amdgcn_readfirstlane(dst + (w + 16) * 1024));
        else glds16(src, (unsigned)__builtin_amdgcn_readfirstlane(dst + w * 1024));
    };
#pragma unroll
    for (int c = 0; c < A_NSLOT - 1; ++c) issue(c);

    const char* qiA = ws + WS_Q + (size_t)head * QIMG;
    const char* qiB = ws + WS_Q + (size_t)(32 + head) * QIMG;
    bf16x8 q1 = *(const bf16x8*)(qiA + h * 32768 + qrow * 16);
    bf16x4 q2 = *(const bf16x4*)(qiA + 65536 + qrow * 8);
    bf16x8 q1b = *(const bf16x8*)(qiB + h * 32768 + qrow * 16);
    bf16x4 q2b = *(const bf16x4*)(qiB + 65536 + qrow * 8);
    if (h) { q2 = (bf16x4){}; q2b = (bf16x4){}; }

    const int vrow = r32 <= 20 ? r32 : (r32 < 28 ? 20 : 19);
    const int k1off = h * 4096 + r32 * 16, k2off = OFF_K2 + r32 * 8, voff = OFF_V + h * 384 + vrow * 16;

    f32x16 o = (f32x16){};
    f32x16 cc;
    for (int c = 0; c < 8; ++c) {
        if (c <= 5) WAIT_BAR(12); else if (c == 6) WAIT_BAR(9); else WAIT_BAR(6);
        if (c + A_NSLOT - 1 < 10) issue(c + A_NSLOT - 1);
        lds_cptr sb = (lds_cptr)lds + (c % A_NSLOT) * CHUNK;
        if (c == 0) {
            f32x16 s = (f32x16){};
            s = MFMA32(lds_b128(sb + k1off), q1, s);
            s = MFMA32K8(lds_b64(sb + k2off), q2, s);
            const float nm = -(rowmax16(s) + MBIAS);
#pragma unroll
            for (int r = 0; r < 16; ++r) cc[r] = nm;
        }
#pragma unroll
        for (int t = 0; t < 8; ++t) {
            bf16x8 k1 = lds_b128(sb + k1off + t * 512);
            bf16x4 k2 = lds_b64(sb + k2off + t * 256);
            bf16x8 v0 = lds_b128(sb + voff + t * 1536);
            bf16x8 v1 = lds_b128(sb + voff + t * 1536 + 768);
            f32x16 s = MFMA32(k1, q1, cc);
            s = MFMA32K8(k2, q2, s);
            softmax_pv(s, v0, v1, o);
        }
    }
    {
        float l = get_l(o);
        if (__builtin_expect(__any(!(l < 1.0f)), 0)) {
            attn_slow(o, kvA, q1, q2, 0, 63, 0, qrow, lane);
            l = get_l(o);
        }
        store_o(o, l, ws + WS_O, head, qrow, h);
    }
    WAIT_BAR(0);
    {
        lds_cptr sb1 = (lds_cptr)lds + (8 % A_NSLOT) * CHUNK;
        lds_cptr sb0 = (lds_cptr)lds + (9 % A_NSLOT) * CHUNK;
        o = (f32x16){};
        float nm;
        {
            f32x16 cm;
#pragma unroll
            for (int r = 0; r < 16; ++r) cm[r] = crow(r, h) <= r32 ? 0.f : -INFINITY;
            f32x16 s = MFMA32(lds_b128(sb1 + k1off + w * 512), q1b, cm);
            s = MFMA32K8(lds_b64(sb1 + k2off + w * 256), q2b, s);
            nm = -(rowmax16(s) + MBIAS);
#pragma unroll
            for (int r = 0; r < 16; ++r) { s[r] += nm; cc[r] = nm; }
            softmax_pv(s, lds_b128(sb1 + voff + w * 1536), lds_b128(sb1 + voff + w * 1536 + 768), o);
        }
        for (int t = 0; t < w; ++t) {
            f32x16 s = MFMA32(lds_b128(sb1 + k1off + t * 512), q1b, cc);
            s = MFMA32K8(lds_b64(sb1 + k2off + t * 256), q2b, s);
            softmax_pv(s, lds_b128(sb1 + voff + t * 1536), lds_b128(sb1 + voff + t * 1536 + 768), o);
        }
        if (j > 0) {
            for (int t = w + 1; t < 8; ++t) {
                f32x16 s = MFMA32(lds_b128(sb0 + k1off + t * 512), q1b, cc);
                s = MFMA32K8(lds_b64(sb0 + k2off + t * 256), q2b, s);
                softmax_pv(s, lds_b128(sb0 + voff + t * 1536), lds_b128(sb0 + voff + t * 1536 + 768), o);
            }
            f32x16 cm;
#pragma unroll
            for (int r = 0; r < 16; ++r) cm[r] = crow(r, h) > r32 ? nm : -INFINITY;
            f32x16 s = MFMA32(lds_b128(sb0 + k1off + w * 512), q1b, cm);
            s = MFMA32K8(lds_b64(sb0 + k2off + w * 256), q2b, s);
            softmax_pv(s, lds_b128(sb0 + voff + w * 1536), lds_b128(sb0 + voff + w * 1536 + 768), o);
        }
        float l = get_l(o);
        if (__builtin_expect(__any(!(l < 1.0f)), 0)) {
            const int ud = j * 8 + w;
            attn_slow(o, kvB, q1b, q2b, ud >= 8 ? ud - 8 : 0, ud, 1, qrow, lane);
            l = get_l(o);
        }
        store_o(o, l, ws + WS_O + WS_O_BYTES / 2, head, qrow, h);
    }
}

constexpr int P_STAGE = 72 * 1024, P_LDS = 2 * P_STAGE;

__global__ __launch_bounds__(512, 2) void oproj_kernel(const char* __restrict__ ws, __hip_bfloat16* __restrict__ out) {
    extern __shared__ __attribute__((aligned(16))) char lds[];
    const int tid = threadIdx.x, lane = tid & 63;
    const int w = __builtin_amdgcn_readfirstlane(tid >> 6);
    const int bid = blockIdx.x;
    const int mb = (bid & 7) * 4 + (bid >> 6), nb = (bid >> 3) & 7;
    const unsigned lds0 = (unsigned)(uintptr_t)lds;
    const char* src[9];
#pragma unroll
    for (int i = 0; i < 9; ++i) {
        const int p = w + 8 * i;
        if (p < 32) {
            const int br = p >> 4, mt = (p >> 2) & 3, ks = p & 3;
            src[i] = ws + WS_O + ((size_t)(br * 128 + mb * 4 + mt) * 20 + ks) * 1024 + lane * 16;
        } else {
            const int pb = p - 32, br = pb / 20, nt = (pb % 20) >> 2, ks = pb & 3;
            src[i] = ws + WS_WO + ((size_t)(br * 40 + nb * 5 + nt) * 20 + ks) * 1024 + lane * 16;
        }
    }
    auto issue = [&](int s) {
        const unsigned base = lds0 + (s & 1) * P_STAGE;
#pragma unroll
        for (int i = 0; i < 9; ++i) glds16(src[i] + s * 4096, (unsigned)__builtin_amdgcn_readfirstlane(base + (w + 8 * i) * 1024));
    };
    issue(0);
    issue(1);
    const int br = w >> 2, mh = (w >> 1) & 1, kp = w & 1;
    f32x4 acc[2][5];
#pragma unroll
    for (int i = 0; i < 2; ++i)
#pragma unroll
        for (int n = 0; n < 5; ++n) acc[i][n] = (f32x4){};
    const int aoff = ((br * 4 + mh * 2) * 4 + kp * 2) * 1024 + lane * 16;
    const int boff = (32 + br * 20 + kp * 2) * 1024 + lane * 16;
    for (int s = 0; s < 5; ++s) {
        if (s < 4) WAIT_BAR(9); else WAIT_BAR(0);
        lds_cptr sb = (lds_cptr)lds + (s & 1) * P_STAGE;
#pragma unroll
        for (int kk = 0; kk < 2; ++kk) {
            bf16x8 af[2], bfr[5];
#pragma unroll
            for (int i = 0; i < 2; ++i) af[i] = lds_b128(sb + aoff + (i * 4 + kk) * 1024);
#pragma unroll
            for (int n = 0; n < 5; ++n) bfr[n] = lds_b128(sb + boff + (n * 4 + kk) * 1024);
#pragma unroll
            for (int i = 0; i < 2; ++i)
#pragma unroll
                for (int n = 0; n < 5; ++n) acc[i][n] = MFMA16(af[i], bfr[n], acc[i][n]);
        }
        if (s + 2 < 5) {
            WAIT_BAR(9);
            issue(s + 2);
        }
    }
    WAIT_BAR(0);
    float* part = (float*)lds + (br * 2 + kp) * 5120;
#pragma unroll
    for (int i = 0; i < 2; ++i)
#pragma unroll
        for (int n = 0; n < 5; ++n)
#pragma unroll
            for (int r = 0; r < 4; ++r) part[(mh * 32 + i * 16 + (lane >> 4) * 4 + r) * 80 + n * 16 + (lane & 15)] = acc[i][n][r];
    __syncthreads();
    const float* gate = (const float*)(ws + WS_G);
    const float* pf = (const float*)lds;
    {
        const int row = tid >> 3, c0 = (tid & 7) * 10;
        const float g = gate[mb * 64 + row];
        unsigned* dst = (unsigned*)(out + (size_t)(mb * 64 + row) * DM + nb * 80 + c0);
#pragma unroll
        for (int i = 0; i < 5; ++i) {
            float v[2];
#pragma unroll
            for (int e = 0; e < 2; ++e) {
                const int idx = row * 80 + c0 + 2 * i + e;
                const float a = pf[idx] + pf[5120 + idx], b = pf[10240 + idx] + pf[15360 + idx];
                v[e] = g * a + (1.0f - g) * b;
            }
            dst[i] = pack2(v[0], v[1]);
        }
    }
}

extern "C" void kernel_launch(void* const* d_in, const int* in_sizes, int n_in, void* d_out, int out_size, void* d_ws, size_t ws_size,
                              hipStream_t stream) {
    const __hip_bfloat16* x = (const __hip_bfloat16*)d_in[0];
    const __hip_bfloat16* wA = (const __hip_bfloat16*)d_in[1];
    const __hip_bfloat16* woA = (const __hip_bfloat16*)d_in[2];
    const __hip_bfloat16* wB = (const __hip_bfloat16*)d_in[4];
    const __hip_bfloat16* woB = (const __hip_bfloat16*)d_in[5];
    const __hip_bfloat16* gw = (const __hip_bfloat16*)d_in[6];
    char* ws = (char*)d_ws;
    (void)hipFuncSetAttribute((const void*)qkv_kernel, hipFuncAttributeMaxDynamicSharedMemorySize, G_LDS);
    (void)hipFuncSetAttribute((const void*)attn_kernel, hipFuncAttributeMaxDynamicSharedMemorySize, A_LDS);
    (void)hipFuncSetAttribute((const void*)oproj_kernel, hipFuncAttributeMaxDynamicSharedMemorySize, P_LDS);
    qkv_kernel<<<256, 512, G_LDS, stream>>>(x, wA, wB, woA, woB, gw, ws);
    attn_kernel<<<256, 512, A_LDS, stream>>>(ws);
    oproj_kernel<<<256, 512, P_LDS, stream>>>(ws, (__hip_bfloat16*)d_out);
}
```

```cpp
#include <hip/hip_runtime.h>
#include <hip/hip_bf16.h>
#include <stdint.h>

typedef __attribute__((ext_vector_type(8))) short bf16x8;
typedef __attribute__((ext_vector_type(4))) short bf16x4;
typedef __attribute__((ext_vector_type(2))) __bf16 bf16v2;
typedef __attribute__((ext_vector_type(2))) float f32x2;
typedef __attribute__((ext_vector_type(16))) float f32x16;
typedef __attribute__((ext_vector_type(4))) float f32x4;
typedef __attribute__((ext_vector_type(4))) unsigned u32x4;
typedef __attribute__((ext_vector_type(2))) unsigned u32x2;
typedef __attribute__((address_space(3))) const char* lds_cptr;
typedef short v4i16_t __attribute__((ext_vector_type(4)));

#define DI __device__ __forceinline__
#define STAMP(i)
#define RSTAMP(i)
#define MFMA32(a, b, c) __builtin_amdgcn_mfma_f32_32x32x16_bf16((a), (b), (c), 0, 0, 0)
#define MFMA32K8(a, b, c) __builtin_amdgcn_mfma_f32_32x32x8bf16_1k((a), (b), (c), 0, 0, 0)
#define MFMA16(a, b, c) __builtin_amdgcn_mfma_f32_16x16x32_bf16((a), (b), (c), 0, 0, 0)
#define SBAR() __builtin_amdgcn_sched_barrier(0)

constexpr int SEQ = 2048, DM = 640, NH = 32, DH = 20, N3 = 1920;
constexpr int CHUNK = 22 * 1024;
constexpr int OFF_V = 10240;
constexpr size_t WS_KV = 0;
constexpr size_t WS_KV_BYTES = (size_t)2 * 32 * 8 * CHUNK;
constexpr int QIMG = 81920;
constexpr size_t WS_Q = WS_KV + WS_KV_BYTES;
constexpr size_t WS_Q_BYTES = (size_t)2 * 32 * QIMG;
constexpr size_t WS_O = WS_Q + WS_Q_BYTES;
constexpr size_t WS_O_BYTES = (size_t)2 * 128 * 20 * 1024;
constexpr size_t WS_WO = WS_O + WS_O_BYTES;
constexpr size_t WS_WO_BYTES = (size_t)2 * 40 * 20 * 1024;
constexpr size_t WS_G = WS_WO + WS_WO_BYTES;

constexpr float QSCALE = 0.22360679774997896f * 1.4426950408889634f;
constexpr float MBIAS = 96.0f;
constexpr float LMAX = 1.2676506e30f;

DI unsigned pack2(float lo, float hi) {
    f32x2 v = {lo, hi};
    bf16v2 b = __builtin_convertvector(v, bf16v2);
    return __builtin_bit_cast(unsigned, b);
}

DI void glds16(const void* g, unsigned lds_base) {
    unsigned sv;
    asm volatile("s_mov_b32 %0, m0\n\ts_mov_b32 m0, %2\n\ts_nop 0\n\tglobal_load_lds_dwordx4 %1, off\n\ts_mov_b32 m0, %0"
                 : "=&s"(sv) : "v"(g), "s"(lds_base) : "memory");
}
#define WAIT_BAR(N) asm volatile("s_waitcnt vmcnt(" #N ") lgkmcnt(0)\n\ts_barrier" ::: "memory")

DI bf16x8 lds_b128(lds_cptr p) { return *(const __attribute__((address_space(3))) bf16x8*)p; }
DI bf16x4 lds_b64(lds_cptr p) { return *(const __attribute__((address_space(3))) bf16x4*)p; }
DI bf16x4 lds_tr(lds_cptr p) {
    return __builtin_bit_cast(bf16x4, __builtin_amdgcn_ds_read_tr16_b64_v4i16((__attribute__((address_space(3))) v4i16_t*)p));
}
DI bf16x8 cat8(bf16x4 lo, bf16x4 hi) { return __builtin_shufflevector(lo, hi, 0, 1, 2, 3, 4, 5, 6, 7); }

constexpr int G_STAGE = 48 * 1024, G_BOFF = 16 * 1024, G_LDS = 3 * G_STAGE;
#define DIAGK 0

template <bool ISV>
DI void qkv_mainloop(f32x16 (&acc)[2][2], char* lds, unsigned lds0, const char* asrc, const char* bsrc, int w, int lane, char* ws) {
    const int r32 = lane & 31, h = lane >> 5;
    const int wm = w & 1, wn = w >> 1;
    const int arow = wm * 64 + r32;
    const int asw = (arow >> 1) & 7;
    int aoff[4];
#pragma unroll
    for (int kk = 0; kk < 4; ++kk) aoff[kk] = arow * 128 + (((2 * kk + h) ^ asw) << 4);
    const int g = lane >> 4, q = (lane & 15) >> 2, p = lane & 3;
    int boff[2];
#pragma unroll
    for (int nt = 0; nt < 2; ++nt)
        boff[nt] = G_BOFF + (8 * (g >> 1) + q) * 512 + (((2 * wn + nt) ^ q) << 6) + (16 * (g & 1) + 4 * p) * 2;

    auto piece = [&](int it, int i) {
        const unsigned base = lds0 + (it % 3) * G_STAGE;
        if (i < 2) glds16(asrc + it * 128 + i * (64 * DM * 2), (unsigned)__builtin_amdgcn_readfirstlane(base + (w + 8 * i) * 1024));
        else glds16(bsrc + (size_t)(it * 64 + (i - 2) * 16) * N3 * 2, (unsigned)__builtin_amdgcn_readfirstlane(base + G_BOFF + (w + 8 * (i - 2)) * 1024));
    };
    auto rd = [&](int it, int kk, bf16x8 (&af)[2], bf16x8 (&bf)[2]) {
        lds_cptr sb = (lds_cptr)lds + (it % 3) * G_STAGE;
#pragma unroll
        for (int mt = 0; mt < 2; ++mt) af[mt] = lds_b128(sb + aoff[kk] + mt * 4096);
#pragma unroll
        for (int nt = 0; nt < 2; ++nt) bf[nt] = cat8(lds_tr(sb + boff[nt] + kk * 8192), lds_tr(sb + boff[nt] + kk * 8192 + 2048));
    };
#pragma unroll
    for (int s = 0; s < 3; ++s)
#pragma unroll
        for (int i = 0; i < 6; ++i) piece(s, i);
    STAMP(1);
    WAIT_BAR(12);
    STAMP(2);
    bf16x8 af[2][2], bf[2][2];
    rd(0, 0, af[0], bf[0]);
#define QMMA(nt, mt) do { if (ISV) acc[nt][mt] = MFMA32(af[cur][mt], bf[cur][nt], acc[nt][mt]); else acc[nt][mt] = MFMA32(bf[cur][nt], af[cur][mt], acc[nt][mt]); } while (0)
#pragma unroll
    for (int it = 0; it < 10; ++it) {
#pragma unroll
        for (int kk = 0; kk < 4; ++kk) {
            const int cur = kk & 1, nxt = cur ^ 1;
            if (kk == 3) {
                if (it <= 7) WAIT_BAR(6); else if (it == 8) WAIT_BAR(0);
                if (it < 9) rd(it + 1, 0, af[nxt], bf[nxt]);
            } else rd(it, kk + 1, af[nxt], bf[nxt]);
            SBAR();
            const int ls = kk == 3 ? it + 3 : it + 2, li = kk == 3 ? 0 : 2 * (kk + 1);
            const bool lv = (kk != 2) && ls < 10 && ls >= 3;
            QMMA(0, 0); QMMA(0, 1); SBAR();
            if (lv) { piece(ls, li); SBAR(); }
            QMMA(1, 0); QMMA(1, 1); SBAR();
            if (lv) { piece(ls, li + 1); SBAR(); }
        }
    }
#undef QMMA
}

__global__ __launch_bounds__(512, 2) void qkv_kernel(const __hip_bfloat16* __restrict__ x, const __hip_bfloat16* __restrict__ wA,
                                                     const __hip_bfloat16* __restrict__ wB, const __hip_bfloat16* __restrict__ woA,
                                                     const __hip_bfloat16* __restrict__ woB, const __hip_bfloat16* __restrict__ gw,
                                                     char* __restrict__ ws) {
    extern __shared__ __attribute__((aligned(16))) char lds[];
    const int tid = threadIdx.x, lane = tid & 63;
    const int w = __builtin_amdgcn_readfirstlane(tid >> 6);
    const int bid = blockIdx.x;
    STAMP(0); RSTAMP(30);
    if (bid >= 240) {
        const int sb = bid - 240;
        const unsigned short* xs = (const unsigned short*)x;
        const unsigned short* gs = (const unsigned short*)gw;
        float* gout = (float*)(ws + WS_G);
        unsigned gv[5], xv[16][5];
#pragma unroll
        for (int i = 0; i < 5; ++i) gv[i] = *(const unsigned*)(gs + lane * 10 + 2 * i);
#pragma unroll
        for (int rr = 0; rr < 16; ++rr)
#pragma unroll
            for (int i = 0; i < 5; ++i) xv[rr][i] = *(const unsigned*)(xs + (size_t)(sb * 128 + w * 16 + rr) * DM + lane * 10 + 2 * i);
        float mine = 0.f;
#pragma unroll
        for (int rr = 0; rr < 16; ++rr) {
            float s = 0.f;
#pragma unroll
            for (int i = 0; i < 5; ++i) {
                s += __uint_as_float(xv[rr][i] << 16) * __uint_as_float(gv[i] << 16);
                s += __uint_as_float(xv[rr][i] & 0xffff0000u) * __uint_as_float(gv[i] & 0xffff0000u);
            }
#pragma unroll
            for (int o = 32; o > 0; o >>= 1) s += __shfl_xor(s, o);
            if (lane == rr) mine = s;
        }
        if (lane < 16) gout[sb * 128 + w * 16 + lane] = 1.0f / (1.0f + __expf(-mine));
        STAMP(1); RSTAMP(31);
        return;
    }
    const int t = (bid & 7) * 30 + (bid >> 3);
    const int tr = t % 120;
    const int n_tile = tr >> 3, m_tile = (t / 120) * 8 + (tr & 7);
    const int m0 = m_tile * 128, n0 = n_tile * 256;
    const unsigned lds0 = (unsigned)(uintptr_t)lds;
    const int wof = t * 8 + w;
    unsigned short we[8];
    {
        const int f = wof < 1600 ? wof : 0;
        const int br_ = f / 800, rem = f % 800, nt = rem / 20, ks = rem % 20;
        const unsigned short* src = (const unsigned short*)(br_ ? woB : woA) + (size_t)(ks * 32 + 8 * (lane >> 4)) * DM + nt * 16 + (lane & 15);
#pragma unroll
        for (int jj = 0; jj < 8; ++jj) we[jj] = src[jj * DM];
    }
    const char* asrc;
    {
        const int row = 8 * w + (lane >> 3);
        const int ch = (lane & 7) ^ ((row >> 1) & 7);
        asrc = (const char*)(x + (size_t)(m0 + row) * DM + ch * 8);
    }
    const char* bsrc;
    {
        const int k = 2 * w + (lane >> 5);
        const int s16 = lane & 31;
        const int c64 = (s16 >> 2) ^ (k & 3);
        const int col = n0 + c64 * 32 + (s16 & 3) * 8;
        const __hip_bfloat16* wp = (col < N3) ? (wA + col) : (wB + (col - N3));
        bsrc = (const char*)(wp + (size_t)k * N3);
    }
    const int wm = w & 1, wn = w >> 1;
    const int ncol = n0 + wn * 64;
    const int br = ncol >= N3 ? 1 : 0;
    const int nb = ncol - br * N3;
    const int part = nb / DM;
    const int np = nb - part * DM;
    f32x16 acc[2][2];
#pragma unroll
    for (int i = 0; i < 2; ++i)
#pragma unroll
        for (int j = 0; j < 2; ++j) acc[i][j] = (f32x16){};
    const int r32 = lane & 31, h = lane >> 5;
    if (part == 2) {
        qkv_mainloop<true>(acc, lds, lds0, asrc, bsrc, w, lane, ws);
        STAMP(3);
#pragma unroll
        for (int nt = 0; nt < 2; ++nt) {
            const int nn = np + nt * 32 + r32;
            const int head = nn / DH, d = nn - head * DH;
#pragma unroll
            for (int mt = 0; mt < 2; ++mt) {
                const int tok0 = m0 + wm * 64 + mt * 32;
                char* base = ws + WS_KV + (size_t)((br * 32 + head) * 8 + (tok0 >> 8)) * CHUNK + OFF_V + ((tok0 >> 5) & 7) * 1536 + h * 384;
#pragma unroll
                for (int s = 0; s < 2; ++s) {
                    const f32x16& a = acc[nt][mt];
                    u32x4 v = {pack2(a[8 * s], a[8 * s + 1]), pack2(a[8 * s + 2], a[8 * s + 3]), pack2(a[8 * s + 4], a[8 * s + 5]),
                               pack2(a[8 * s + 6], a[8 * s + 7])};
                    *(u32x4*)(base + s * 768 + d * 16) = v;
                    if (d == 0) *(u32x4*)(base + s * 768 + 320) = (u32x4){0x3f803f80u, 0x3f803f80u, 0x3f803f80u, 0x3f803f80u};
                }
            }
        }
    } else {
        qkv_mainloop<false>(acc, lds, lds0, asrc, bsrc, w, lane, ws);
        STAMP(3);
        const float sc = part == 0 ? QSCALE : 1.0f;
        const int tok = m0 + wm * 64 + r32;
        char* img = part == 0 ? ws + WS_Q + (size_t)br * 32 * QIMG + tok * 8
                              : ws + WS_KV + (size_t)(br * 256 + (tok >> 8)) * CHUNK + (tok & 255) * 8;
        const int hstride = part == 0 ? QIMG : 8 * CHUNK, gstride = part == 0 ? 16384 : 2048;
#pragma unroll
        for (int nt = 0; nt < 2; ++nt) {
#pragma unroll
            for (int rg = 0; rg < 4; ++rg) {
                const int nn = np + nt * 32 + 8 * rg + 4 * h;
                const int head = nn / DH, gq = (nn - head * DH) >> 2;
                char* dst = img + (size_t)head * hstride + gq * gstride;
#pragma unroll
                for (int mt = 0; mt < 2; ++mt) {
                    const f32x16& a = acc[nt][mt];
                    u32x2 v = {pack2(a[4 * rg] * sc, a[4 * rg + 1] * sc), pack2(a[4 * rg + 2] * sc, a[4 * rg + 3] * sc)};
                    *(u32x2*)(dst + mt * 256) = v;
                }
            }
        }
    }
    if (wof < 1600) {
        u32x4 v;
#pragma unroll
        for (int jj = 0; jj < 4; ++jj) v[jj] = (unsigned)we[2 * jj] | ((unsigned)we[2 * jj + 1] << 16);
        *(u32x4*)(ws + WS_WO + (size_t)wof * 1024 + lane * 16) = v;
    }
    STAMP(4); RSTAMP(31);
}
#undef DIAGK

constexpr int A_NSLOT = 6, A_LDS = A_NSLOT * CHUNK;
#define DIAGK 1

DI float half_swap_max(float m) {
    auto rr = __builtin_amdgcn_permlane32_swap(__float_as_uint(m), __float_as_uint(m), false, false);
    return __builtin_fmaxf(__uint_as_float(rr[0]), __uint_as_float(rr[1]));
}
DI float rowmax16(const f32x16& s) {
    float a = __builtin_fmaxf(__builtin_fmaxf(s[0], s[1]), s[2]);
#pragma unroll
    for (int r = 3; r < 15; r += 2) a = __builtin_fmaxf(__builtin_fmaxf(a, s[r]), s[r + 1]);
    a = __builtin_fmaxf(a, s[15]);
    return half_swap_max(a);
}
DI void softmax_pv(const f32x16& s, const bf16x8& v0, const bf16x8& v1, f32x16& o) {
    float p[16];
#pragma unroll
    for (int r = 0; r < 16; ++r) p[r] = __builtin_amdgcn_exp2f(s[r]);
    u32x4 p0 = {pack2(p[0], p[1]), pack2(p[2], p[3]), pack2(p[4], p[5]), pack2(p[6], p[7])};
    u32x4 p1 = {pack2(p[8], p[9]), pack2(p[10], p[11]), pack2(p[12], p[13]), pack2(p[14], p[15])};
    o = MFMA32(v0, __builtin_bit_cast(bf16x8, p0), o);
    o = MFMA32(v1, __builtin_bit_cast(bf16x8, p1), o);
}
DI int crow(int r, int hi) { return (r & 3) + 8 * (r >> 2) + 4 * hi; }

DI void attn_slow(f32x16& o, const char* kv, const bf16x8 q1, const bf16x4 q2, int ubeg, int uend, int mode, int qpos, int lane) {
    const int r32 = lane & 31, h = lane >> 5;
    const int vrow = r32 <= 20 ? r32 : 20;
    float m = -INFINITY;
    o = (f32x16){};
    for (int i = uend; i >= ubeg; --i) {
        const char* cb = kv + (size_t)(i >> 3) * CHUNK;
        const int t = i & 7;
        const char* kp = cb + (t * 32 + r32) * 8;
        bf16x8 k1 = cat8(*(const bf16x4*)(kp + h * 4096), *(const bf16x4*)(kp + h * 4096 + 2048));
        bf16x4 k2 = *(const bf16x4*)(kp + 8192);
        bf16x8 v0 = *(const bf16x8*)(cb + OFF_V + t * 1536 + h * 384 + vrow * 16);
        bf16x8 v1 = *(const bf16x8*)(cb + OFF_V + t * 1536 + 768 + h * 384 + vrow * 16);
        f32x16 s = (f32x16){};
        s = MFMA32(k1, q1, s);
        s = MFMA32K8(k2, q2, s);
        if (mode) {
#pragma unroll
            for (int r = 0; r < 16; ++r) {
                const int dlt = qpos - (i * 32 + crow(r, h));
                if (dlt < 0 || dlt >= 256) s[r] = -INFINITY;
            }
        }
        const float mn = __builtin_fmaxf(m, rowmax16(s));
        const float alpha = __builtin_amdgcn_exp2f(m - mn);
        m = mn;
#pragma unroll
        for (int r = 0; r < 16; ++r) { o[r] *= alpha; s[r] -= mn; }
        softmax_pv(s, v0, v1, o);
    }
}

DI void store_o(const f32x16& o, float l, char* oimg, int head, int qrow, int h) {
    const float rl = 1.0f / l;
    char* base = oimg + (size_t)(qrow >> 4) * 20 * 1024 + (qrow & 15) * 16;
#pragma unroll
    for (int rg = 0; rg < 3; ++rg) {
        const int d0 = 8 * rg + 4 * h;
        if (d0 < DH) {
            const int k = head * DH + d0;
            u32x2 v = {pack2(o[4 * rg] * rl, o[4 * rg + 1] * rl), pack2(o[4 * rg + 2] * rl, o[4 * rg + 3] * rl)};
            *(u32x2*)(base + (k >> 5) * 1024 + ((k >> 3) & 3) * 256 + ((k >> 2) & 1) * 8) = v;
        }
    }
}
DI float get_l(const f32x16& o) {
    auto rr = __builtin_amdgcn_permlane32_swap(__float_as_uint(o[8]), __float_as_uint(o[8]), false, false);
    return __uint_as_float(rr[1]);
}
DI bool unsafe(const f32x16& o, float l) {
    float z = 0.f;
#pragma unroll
    for (int r = 0; r < 12; ++r) z = __builtin_fmaf(o[r], 0.f, z);
    return __any(!(l < LMAX) || !(z == 0.f));
}
#define EXP4(P, S, i) do { P[i] = __builtin_amdgcn_exp2f(S[i]); P[i + 1] = __builtin_amdgcn_exp2f(S[i + 1]); \
                           P[i + 2] = __builtin_amdgcn_exp2f(S[i + 2]); P[i + 3] = __builtin_amdgcn_exp2f(S[i + 3]); } while (0)
#define STEP(SC, SN, CN, Q1, Q2, KP, VP, VPN) do { \
    float p_[12]; \
    SN = MFMA32(cat8(k1a, k1b), Q1, CN); SBAR(); \
    k1a = lds_b64(KP); k1b = lds_b64((KP) + 2048); \
    EXP4(p_, SC, 0); pb[2] = pack2(pt[0], pt[1]); pb[3] = pack2(pt[2], pt[3]); SBAR(); \
    o = MFMA32(v1, __builtin_bit_cast(bf16x8, pb), o); SBAR(); \
    v1 = lds_b128((VP) + 768); \
    EXP4(p_, SC, 4); pa[0] = pack2(p_[0], p_[1]); pa[1] = pack2(p_[2], p_[3]); SBAR(); \
    SN = MFMA32K8(k2, Q2, SN); SBAR(); \
    k2 = lds_b64((KP) + k2d); \
    EXP4(p_, SC, 8); pa[2] = pack2(p_[4], p_[5]); pa[3] = pack2(p_[6], p_[7]); SBAR(); \
    o = MFMA32(v0, __builtin_bit_cast(bf16x8, pa), o); SBAR(); \
    v0 = lds_b128(VPN); \
    pt[0] = __builtin_amdgcn_exp2f(SC[12]); pt[1] = __builtin_amdgcn_exp2f(SC[13]); \
    pt[2] = __builtin_amdgcn_exp2f(SC[14]); pt[3] = __builtin_amdgcn_exp2f(SC[15]); \
    pb[0] = pack2(p_[8], p_[9]); pb[1] = pack2(p_[10], p_[11]); SBAR(); \
    } while (0)
#define DRAIN() do { pb[2] = pack2(pt[0], pt[1]); pb[3] = pack2(pt[2], pt[3]); o = MFMA32(v1, __builtin_bit_cast(bf16x8, pb), o); } while (0)

__global__ __launch_bounds__(512, 2) void attn_kernel(char* __restrict__ ws) {
    extern __shared__ __attribute__((aligned(16))) char lds[];
    const int tid = threadIdx.x, lane = tid & 63, r32 = lane & 31, h = lane >> 5;
    const int w = __builtin_amdgcn_readfirstlane(tid >> 6);
    const int bid = blockIdx.x;
    const int head = (bid & 7) * 4 + (bid >> 6), j = (bid >> 3) & 7;
    const unsigned lds0 = (unsigned)(uintptr_t)lds;
    const char* kvA = ws + WS_KV + (size_t)(head * 8) * CHUNK;
    const char* kvB = ws + WS_KV + (size_t)((32 + head) * 8) * CHUNK;
    const int qrow = j * 256 + w * 32 + r32;
    STAMP(0); RSTAMP(30);
    const int p3 = w < 6 ? 16384 : 0;
    auto issue = [&](int c) {
        const char* src = c < 8 ? kvA + (size_t)c * CHUNK : kvB + (size_t)(c == 8 ? j : (j > 0 ? j - 1 : 0)) * CHUNK;
        const unsigned dst = lds0 + (c % A_NSLOT) * CHUNK + w * 1024;
        src += w * 1024 + lane * 16;
        glds16(src, (unsigned)__builtin_amdgcn_readfirstlane(dst));
        glds16(src + 8192, (unsigned)__builtin_amdgcn_readfirstlane(dst + 8192));
        glds16(src + p3, (unsigned)__builtin_amdgcn_readfirstlane(dst + p3));
    };
    const char* qp = ws + WS_Q + (size_t)head * QIMG + qrow * 8;
    bf16x4 qa, qb, q2;
    asm volatile("global_load_dwordx2 %0, %3, off\n\tglobal_load_dwordx2 %1, %4, off\n\tglobal_load_dwordx2 %2, %5, off"
                 : "=&v"(qa), "=&v"(qb), "=&v"(q2) : "v"(qp + h * 32768), "v"(qp + h * 32768 + 16384), "v"(qp + 65536) : "memory");
#pragma unroll
    for (int c = 0; c < A_NSLOT; ++c) issue(c);
    asm volatile("s_waitcnt vmcnt(15) lgkmcnt(0)\n\ts_barrier" : "+v"(qa), "+v"(qb), "+v"(q2) :: "memory");
    STAMP(1);
    bf16x8 q1 = cat8(qa, qb);
    if (h) q2 = (bf16x4){};

    const int vrow = r32 <= 20 ? r32 : (r32 < 28 ? 20 : 19);
    const int koff = h * 4096 + r32 * 8, k2d = 8192 - h * 4096, voff = OFF_V + h * 384 + vrow * 16;

    f32x16 o = (f32x16){};
    f32x16 cc, sa, sb_;
    bf16x8 v0, v1;
    bf16x4 k1a, k1b, k2;
    u32x4 pa, pb = (u32x4){};
    float pt[4] = {0.f, 0.f, 0.f, 0.f};
    {
        lds_cptr kp = (lds_cptr)lds + koff;
        f32x16 s = (f32x16){};
        s = MFMA32(cat8(lds_b64(kp), lds_b64(kp + 2048)), q1, s);
        s = MFMA32K8(lds_b64(kp + k2d), q2, s);
        const float nm = -(rowmax16(s) + MBIAS);
#pragma unroll
        for (int r = 0; r < 16; ++r) { cc[r] = nm; sa[r] = s[r] + nm; }
        k1a = lds_b64(kp + 256); k1b = lds_b64(kp + 256 + 2048); k2 = lds_b64(kp + 256 + k2d);
        v0 = lds_b128((lds_cptr)lds + voff);
        v1 = v0;
    }
    int slot = 0;
    for (int c = 0; c < 8; ++c) {
        const int nslot = slot == A_NSLOT - 1 ? 0 : slot + 1;
        lds_cptr kc = (lds_cptr)lds + slot * CHUNK + koff, vc = (lds_cptr)lds + slot * CHUNK + voff;
        lds_cptr kn = (lds_cptr)lds + nslot * CHUNK + koff, vn = (lds_cptr)lds + nslot * CHUNK + voff;
        __builtin_amdgcn_s_setprio(2);
        STEP(sa, sb_, cc, q1, q2, kc + 2 * 256, vc + 0 * 1536, vc + 1 * 1536);
        STEP(sb_, sa, cc, q1, q2, kc + 3 * 256, vc + 1 * 1536, vc + 2 * 1536);
        __builtin_amdgcn_s_setprio(1);
        STEP(sa, sb_, cc, q1, q2, kc + 4 * 256, vc + 2 * 1536, vc + 3 * 1536);
        STEP(sb_, sa, cc, q1, q2, kc + 5 * 256, vc + 3 * 1536, vc + 4 * 1536);
        __builtin_amdgcn_s_setprio(0);
        STEP(sa, sb_, cc, q1, q2, kc + 6 * 256, vc + 4 * 1536, vc + 5 * 1536);
        STEP(sb_, sa, cc, q1, q2, kc + 7 * 256, vc + 5 * 1536, vc + 6 * 1536);
        if (c == 0) WAIT_BAR(12); else if (c <= 5) WAIT_BAR(9); else WAIT_BAR(6);
        if (c >= 1 && c + 5 < 10) issue(c + 5);
        __builtin_amdgcn_s_setprio(3);
        STEP(sa, sb_, cc, q1, q2, kn + 0 * 256, vc + 6 * 1536, vc + 7 * 1536);
        STEP(sb_, sa, cc, q1, q2, kn + 1 * 256, vc + 7 * 1536, vn + 0 * 1536);
        slot = nslot;
    }
    DRAIN();
    STAMP(2);
    bf16x8 q1b;
    bf16x4 q2b;
    {
        const char* qpb = qp + (size_t)32 * QIMG;
        q1b = cat8(*(const bf16x4*)(qpb + h * 32768), *(const bf16x4*)(qpb + h * 32768 + 16384));
        q2b = *(const bf16x4*)(qpb + 65536);
    }
    {
        float l = get_l(o);
        if (__builtin_expect(unsafe(o, l), 0)) {
            attn_slow(o, kvA, q1, q2, 0, 63, 0, qrow, lane);
            l = get_l(o);
        }
        store_o(o, l, ws + WS_O, head, qrow, h);
    }
    if (h) q2b = (bf16x4){};
    STAMP(3);
    WAIT_BAR(0);
    STAMP(4);
    {
        constexpr int S1 = (8 % A_NSLOT) * CHUNK, S0 = (9 % A_NSLOT) * CHUNK;
        lds_cptr kb = (lds_cptr)lds + koff, vb = (lds_cptr)lds + voff;
        auto kad = [&](int u) { return kb + ((u < 8 ? S0 : S1) + (u & 7) * 256); };
        auto vad = [&](int u) { return vb + ((u < 8 ? S0 : S1) + (u & 7) * 1536); };
        o = (f32x16){};
        float nm;
        {
            f32x16 cm;
#pragma unroll
            for (int r = 0; r < 16; ++r) cm[r] = crow(r, h) <= r32 ? 0.f : -INFINITY;
            lds_cptr kp = kad(w + 8);
            f32x16 s = MFMA32(cat8(lds_b64(kp), lds_b64(kp + 2048)), q1b, cm);
            s = MFMA32K8(lds_b64(kp + k2d), q2b, s);
            nm = -(rowmax16(s) + MBIAS);
#pragma unroll
            for (int r = 0; r < 16; ++r) { sa[r] = s[r] + nm; cc[r] = nm; }
        }
        if (j > 0) {
            f32x16 cf;
#pragma unroll
            for (int r = 0; r < 16; ++r) cf[r] = crow(r, h) > r32 ? nm : -INFINITY;
            pb = (u32x4){};
            pt[0] = pt[1] = pt[2] = pt[3] = 0.f;
            { lds_cptr kp = kad(w + 7); k1a = lds_b64(kp); k1b = lds_b64(kp + 2048); k2 = lds_b64(kp + k2d); }
            v0 = lds_b128(vad(w + 8));
            v1 = v0;
            STEP(sa, sb_, cc, q1b, q2b, kad(w + 6), vad(w + 8), vad(w + 7));
            STEP(sb_, sa, cc, q1b, q2b, kad(w + 5), vad(w + 7), vad(w + 6));
            STEP(sa, sb_, cc, q1b, q2b, kad(w + 4), vad(w + 6), vad(w + 5));
            STEP(sb_, sa, cc, q1b, q2b, kad(w + 3), vad(w + 5), vad(w + 4));
            STEP(sa, sb_, cc, q1b, q2b, kad(w + 2), vad(w + 4), vad(w + 3));
            STEP(sb_, sa, cc, q1b, q2b, kad(w + 1), vad(w + 3), vad(w + 2));
            STEP(sa, sb_, cc, q1b, q2b, kad(w), vad(w + 2), vad(w + 1));
            STEP(sb_, sa, cf, q1b, q2b, kad(w), vad(w + 1), vad(w));
            STEP(sa, sb_, cc, q1b, q2b, kad(w), vad(w), vad(w));
            DRAIN();
        } else {
            softmax_pv(sa, lds_b128(vad(w + 8)), lds_b128(vad(w + 8) + 768), o);
            for (int u = 8; u < w + 8; ++u) {
                lds_cptr kp = kad(u);
                f32x16 s = MFMA32(cat8(lds_b64(kp), lds_b64(kp + 2048)), q1b, cc);
                s = MFMA32K8(lds_b64(kp + k2d), q2b, s);
                softmax_pv(s, lds_b128(vad(u)), lds_b128(vad(u) + 768), o);
            }
        }
        float l = get_l(o);
        if (__builtin_expect(unsafe(o, l), 0)) {
            const int ud = j * 8 + w;
            attn_slow(o, kvB, q1b, q2b, ud >= 8 ? ud - 8 : 0, ud, 1, qrow, lane);
            l = get_l(o);
        }
        store_o(o, l, ws + WS_O + WS_O_BYTES / 2, head, qrow, h);
    }
    STAMP(5); RSTAMP(31);
}
#undef DIAGK

constexpr int P_STAGE = 36 * 1024, P_LDS = 4 * P_STAGE;
#define DIAGK 2

__global__ __launch_bounds__(512, 2) void oproj_kernel(char* __restrict__ ws, __hip_bfloat16* __restrict__ out) {
    extern __shared__ __attribute__((aligned(16))) char lds[];
    const int tid = threadIdx.x, lane = tid & 63;
    const int w = __builtin_amdgcn_readfirstlane(tid >> 6);
    const int bid = blockIdx.x;
    const int mb = (bid & 7) * 4 + (bid >> 6), nb = (bid >> 3) & 7;
    const unsigned lds0 = (unsigned)(uintptr_t)lds;
    STAMP(0); RSTAMP(30);
    const char* src[5];
#pragma unroll
    for (int i = 0; i < 5; ++i) {
        int p = w + 8 * i;
        if (p >= 36) p = w;
        if (p < 16) {
            const int br = p >> 3, mt = (p >> 1) & 3, ks = p & 1;
            src[i] = ws + WS_O + ((size_t)(br * 128 + mb * 4 + mt) * 20 + ks) * 1024 + lane * 16;
        } else {
            const int pb = p - 16, br = pb / 10, nt = (pb % 10) >> 1, ks = pb & 1;
            src[i] = ws + WS_WO + ((size_t)(br * 40 + nb * 5 + nt) * 20 + ks) * 1024 + lane * 16;
        }
    }
    auto piece = [&](int s, int i) {
        if (i < 4 || w < 4) glds16(src[i] + s * 2048, (unsigned)__builtin_amdgcn_readfirstlane(lds0 + (s & 3) * P_STAGE + (w + 8 * i) * 1024));
    };
#pragma unroll
    for (int s = 0; s < 3; ++s)
#pragma unroll
        for (int i = 0; i < 5; ++i) piece(s, i);
    const float g = ((const float*)(ws + WS_G))[mb * 64 + (tid >> 3)];
    const int br = w >> 2, mh = (w >> 1) & 1, kp = w & 1;
    f32x4 acc[2][5];
#pragma unroll
    for (int i = 0; i < 2; ++i)
#pragma unroll
        for (int n = 0; n < 5; ++n) acc[i][n] = (f32x4){};
    const int aoff = ((br * 4 + mh * 2) * 2 + kp) * 1024 + lane * 16;
    const int boff = (16 + br * 10 + kp) * 1024 + lane * 16;
    STAMP(1);
#pragma unroll
    for (int s = 0; s < 10; ++s) {
        if (s <= 7) { if (w < 4) WAIT_BAR(10); else WAIT_BAR(8); }
        else if (s == 8) { if (w < 4) WAIT_BAR(5); else WAIT_BAR(4); }
        else WAIT_BAR(0);
        lds_cptr sb = (lds_cptr)lds + (s & 3) * P_STAGE;
        bf16x8 af[2], bfr[5];
#pragma unroll
        for (int i = 0; i < 2; ++i) af[i] = lds_b128(sb + aoff + i * 2048);
#pragma unroll
        for (int n = 0; n < 5; ++n) bfr[n] = lds_b128(sb + boff + n * 2048);
        if (s + 3 < 10) {
#pragma unroll
            for (int i = 0; i < 5; ++i) piece(s + 3, i);
        }
#pragma unroll
        for (int i = 0; i < 2; ++i)
#pragma unroll
            for (int n = 0; n < 5; ++n) acc[i][n] = MFMA16(af[i], bfr[n], acc[i][n]);
    }
    STAMP(2);
    WAIT_BAR(0);
    float* part = (float*)lds + (br * 2 + kp) * 5120;
#pragma unroll
    for (int i = 0; i < 2; ++i)
#pragma unroll
        for (int n = 0; n < 5; ++n)
#pragma unroll
            for (int r = 0; r < 4; ++r) part[(mh * 32 + i * 16 + (lane >> 4) * 4 + r) * 80 + n * 16 + (lane & 15)] = acc[i][n][r];
    __syncthreads();
    const float* pf = (const float*)lds;
    {
        const int row = tid >> 3, c0 = (tid & 7) * 10;
        unsigned* dst = (unsigned*)(out + (size_t)(mb * 64 + row) * DM + nb * 80 + c0);
#pragma unroll
        for (int i = 0; i < 5; ++i) {
            float v[2];
#pragma unroll
            for (int e = 0; e < 2; ++e) {
                const int idx = row * 80 + c0 + 2 * i + e;
                const float a = pf[idx] + pf[5120 + idx], b = pf[10240 + idx] + pf[15360 + idx];
                v[e] = g * a + (1.0f - g) * b;
            }
            dst[i] = pack2(v[0], v[1]);
        }
    }
    STAMP(3); RSTAMP(31);
}
#undef DIAGK

extern "C" void kernel_launch(void* const* d_in, const int* in_sizes, int n_in, void* d_out, int out_size, void* d_ws, size_t ws_size,
                              hipStream_t stream) {
    const __hip_bfloat16* x = (const __hip_bfloat16*)d_in[0];
    const __hip_bfloat16* wA = (const __hip_bfloat16*)d_in[1];
    const __hip_bfloat16* woA = (const __hip_bfloat16*)d_in[2];
    const __hip_bfloat16* wB = (const __hip_bfloat16*)d_in[4];
    const __hip_bfloat16* woB = (const __hip_bfloat16*)d_in[5];
    const __hip_bfloat16* gw = (const __hip_bfloat16*)d_in[6];
    char* ws = (char*)d_ws;
    (void)hipFuncSetAttribute((const void*)qkv_kernel, hipFuncAttributeMaxDynamicSharedMemorySize, G_LDS);
    (void)hipFuncSetAttribute((const void*)attn_kernel, hipFuncAttributeMaxDynamicSharedMemorySize, A_LDS);
    (void)hipFuncSetAttribute((const void*)oproj_kernel, hipFuncAttributeMaxDynamicSharedMemorySize, P_LDS);
    qkv_kernel<<<256, 512, G_LDS, stream>>>(x, wA, wB, woA, woB, gw, ws);
    attn_kernel<<<256, 512, A_LDS, stream>>>(ws);
    oproj_kernel<<<256, 512, P_LDS, stream>>>(ws, (__hip_bfloat16*)d_out);
}
```

```cpp
#include <hip/hip_runtime.h>
#include <hip/hip_bf16.h>
#include <stdint.h>

typedef __attribute__((ext_vector_type(8))) short bf16x8;
typedef __attribute__((ext_vector_type(4))) short bf16x4;
typedef __attribute__((ext_vector_type(2))) __bf16 bf16v2;
typedef __attribute__((ext_vector_type(2))) float f32x2;
typedef __attribute__((ext_vector_type(16))) float f32x16;
typedef __attribute__((ext_vector_type(4))) float f32x4;
typedef __attribute__((ext_vector_type(4))) unsigned u32x4;
typedef __attribute__((ext_vector_type(2))) unsigned u32x2;
typedef __attribute__((address_space(3))) const char* lds_cptr;
typedef short v4i16_t __attribute__((ext_vector_type(4)));
typedef __attribute__((address_space(1))) unsigned gu32;
typedef __attribute__((address_space(1))) unsigned long long gu64;

#define DI __device__ __forceinline__
#define STAMP(i)
#define RSTAMP(i)
#define MFMA32(a, b, c) __builtin_amdgcn_mfma_f32_32x32x16_bf16((a), (b), (c), 0, 0, 0)
#define MFMA32K8(a, b, c) __builtin_amdgcn_mfma_f32_32x32x8bf16_1k((a), (b), (c), 0, 0, 0)
#define MFMA16(a, b, c) __builtin_amdgcn_mfma_f32_16x16x32_bf16((a), (b), (c), 0, 0, 0)
#define SBAR() __builtin_amdgcn_sched_barrier(0)

constexpr int SEQ = 2048, DM = 640, NH = 32, DH = 20, N3 = 1920;
constexpr int CHUNK = 22 * 1024;
constexpr int OFF_V = 10240;
constexpr size_t WS_KV = 0;
constexpr size_t WS_KV_BYTES = (size_t)2 * 32 * 8 * CHUNK;
constexpr int QIMG = 81920;
constexpr size_t WS_Q = WS_KV + WS_KV_BYTES;
constexpr size_t WS_Q_BYTES = (size_t)2 * 32 * QIMG;
constexpr size_t WS_O = WS_Q + WS_Q_BYTES;
constexpr size_t WS_O_BYTES = (size_t)2 * 128 * 20 * 1024;
constexpr size_t WS_WO = WS_O + WS_O_BYTES;
constexpr size_t WS_WO_BYTES = (size_t)2 * 40 * 20 * 1024;
constexpr size_t WS_G = WS_WO + WS_WO_BYTES;
constexpr size_t WS_CNT = WS_G + 8192;

constexpr float QSCALE = 0.22360679774997896f * 1.4426950408889634f;
constexpr float MBIAS = 96.0f;
constexpr float LMAX = 1.2676506e30f;

DI unsigned pack2(float lo, float hi) {
    f32x2 v = {lo, hi};
    bf16v2 b = __builtin_convertvector(v, bf16v2);
    return __builtin_bit_cast(unsigned, b);
}

DI void glds16(const void* g, unsigned lds_base) {
    unsigned sv;
    asm volatile("s_mov_b32 %0, m0\n\ts_mov_b32 m0, %2\n\ts_nop 0\n\tglobal_load_lds_dwordx4 %1, off\n\ts_mov_b32 m0, %0"
                 : "=&s"(sv) : "v"(g), "s"(lds_base) : "memory");
}
#define WAIT_BAR(N) asm volatile("s_waitcnt vmcnt(" #N ") lgkmcnt(0)\n\ts_barrier" ::: "memory")

DI bf16x8 lds_b128(lds_cptr p) { return *(const __attribute__((address_space(3))) bf16x8*)p; }
DI bf16x4 lds_b64(lds_cptr p) { return *(const __attribute__((address_space(3))) bf16x4*)p; }
DI bf16x4 lds_tr(lds_cptr p) {
    return __builtin_bit_cast(bf16x4, __builtin_amdgcn_ds_read_tr16_b64_v4i16((__attribute__((address_space(3))) v4i16_t*)p));
}
DI bf16x8 cat8(bf16x4 lo, bf16x4 hi) { return __builtin_shufflevector(lo, hi, 0, 1, 2, 3, 4, 5, 6, 7); }

constexpr int G_STAGE = 48 * 1024, G_BOFF = 16 * 1024, G_LDS = 3 * G_STAGE;

template <bool ISV>
#define DIAGK 0
DI void gemm_consume(f32x16 (&acc)[4][2], char* lds, int w, int lane, char* ws) {
    const int r32 = lane & 31, h = lane >> 5;
    const int wm = w & 1, wn = w >> 1;
    const int arow = wm * 64 + r32;
    const int asw = (arow >> 1) & 7;
    int aoff[4];
#pragma unroll
    for (int kk = 0; kk < 4; ++kk) aoff[kk] = arow * 128 + (((2 * kk + h) ^ asw) << 4);
    const int g = lane >> 4, q = (lane & 15) >> 2, p = lane & 3;
    int boff[4];
#pragma unroll
    for (int nt = 0; nt < 4; ++nt) boff[nt] = G_BOFF + (8 * (g >> 1) + q) * 512 + (((4 * wn + nt) ^ q) << 6) + (16 * (g & 1) + 4 * p) * 2;
    bf16x8 a0[2], b0[4], a1[2], b1[4];
#define MM(A, B, nt, mt) do { if (ISV) acc[nt][mt] = MFMA32(A[mt], B[nt], acc[nt][mt]); else acc[nt][mt] = MFMA32(B[nt], A[mt], acc[nt][mt]); } while (0)
#define RB(BN, sb, kk, nt) BN[nt] = cat8(lds_tr(sb + boff[nt] + (kk) * 8192), lds_tr(sb + boff[nt] + (kk) * 8192 + 2048))
#define RA(AN, sb, kk) AN[0] = lds_b128(sb + aoff[kk]), AN[1] = lds_b128(sb + aoff[kk] + 4096)
#define GMMA(A, B, AN, BN, sb, kk) do { \
        MM(A, B, 0, 0); SBAR(); RA(AN, sb, kk); SBAR(); \
        MM(A, B, 0, 1); SBAR(); RB(BN, sb, kk, 0); SBAR(); \
        MM(A, B, 1, 0); SBAR(); RB(BN, sb, kk, 1); SBAR(); \
        MM(A, B, 1, 1); SBAR(); RB(BN, sb, kk, 2); SBAR(); \
        MM(A, B, 2, 0); SBAR(); RB(BN, sb, kk, 3); SBAR(); \
        MM(A, B, 2, 1); MM(A, B, 3, 0); MM(A, B, 3, 1); SBAR(); } while (0)
    WAIT_BAR(0);
    STAMP(1);
    {
        lds_cptr sb = (lds_cptr)lds;
        a0[0] = lds_b128(sb + aoff[0]); a0[1] = lds_b128(sb + aoff[0] + 4096);
#pragma unroll
        for (int nt = 0; nt < 4; ++nt) b0[nt] = cat8(lds_tr(sb + boff[nt]), lds_tr(sb + boff[nt] + 2048));
    }
    int sl = 0;
#pragma unroll 1
    for (int it = 0; it < 10; ++it) {
        const int nsl = sl == 2 ? 0 : sl + 1;
        lds_cptr sc = (lds_cptr)lds + sl * G_STAGE, sn = (lds_cptr)lds + nsl * G_STAGE;
        GMMA(a0, b0, a1, b1, sc, 1);
        GMMA(a1, b1, a0, b0, sc, 2);
        GMMA(a0, b0, a1, b1, sc, 3);
        if (it < 9) WAIT_BAR(0);
        GMMA(a1, b1, a0, b0, sn, 0);
        sl = nsl;
    }
#undef MM
#undef RB
#undef RA
#undef GMMA
}

DI void gemm_stage(const char* asrc, const char* bsrc, unsigned lds0, int lw) {
    auto issue = [&](int s, int sl) {
        const unsigned base = lds0 + sl * G_STAGE + lw * 1024;
#pragma unroll
        for (int i = 0; i < 4; ++i) glds16(asrc + s * 128 + i * (32 * DM * 2), (unsigned)__builtin_amdgcn_readfirstlane(base + i * 4096));
#pragma unroll
        for (int i = 0; i < 8; ++i) glds16(bsrc + (size_t)(s * 64 + i * 8) * N3 * 2, (unsigned)__builtin_amdgcn_readfirstlane(base + G_BOFF + i * 4096));
    };
    issue(0, 0);
    issue(1, 1);
    WAIT_BAR(12);
    issue(2, 2);
    int sl = 0;
#pragma unroll 1
    for (int it = 0; it < 9; ++it) {
        if (it <= 7) WAIT_BAR(12); else WAIT_BAR(0);
        if (it + 3 < 10) issue(it + 3, sl);
        sl = sl == 2 ? 0 : sl + 1;
    }
}

DI void gate_job(const __hip_bfloat16* __restrict__ x, const __hip_bfloat16* __restrict__ gw, char* __restrict__ ws, int sb, int w, int lane) {
    const unsigned short* xs = (const unsigned short*)x;
    const unsigned short* gs = (const unsigned short*)gw;
    float* gout = (float*)(ws + WS_G);
    unsigned gv[5];
#pragma unroll
    for (int i = 0; i < 5; ++i) gv[i] = *(const unsigned*)(gs + lane * 10 + 2 * i);
    float mine = 0.f;
#pragma unroll
    for (int r4 = 0; r4 < 16; r4 += 8) {
        unsigned xv[8][5];
#pragma unroll
        for (int rr = 0; rr < 8; ++rr)
#pragma unroll
            for (int i = 0; i < 5; ++i) xv[rr][i] = *(const unsigned*)(xs + (size_t)(sb * 128 + w * 16 + r4 + rr) * DM + lane * 10 + 2 * i);
#pragma unroll
        for (int rr = 0; rr < 8; ++rr) {
            float s = 0.f;
#pragma unroll
            for (int i = 0; i < 5; ++i) {
                s += __uint_as_float(xv[rr][i] << 16) * __uint_as_float(gv[i] << 16);
                s += __uint_as_float(xv[rr][i] & 0xffff0000u) * __uint_as_float(gv[i] & 0xffff0000u);
            }
#pragma unroll
            for (int o = 32; o > 0; o >>= 1) s += __shfl_xor(s, o);
            if (lane == r4 + rr) mine = s;
        }
    }
    if (lane < 16) gout[sb * 128 + w * 16 + lane] = 1.0f / (1.0f + __expf(-mine));
}
DI void wo_job(const __hip_bfloat16* __restrict__ woA, const __hip_bfloat16* __restrict__ woB, char* __restrict__ ws, int f, int lane) {
    const int br_ = f / 800, rem = f % 800, nt = rem / 20, ks = rem % 20;
    const unsigned short* src = (const unsigned short*)(br_ ? woB : woA) + (size_t)(ks * 32 + 8 * (lane >> 4)) * DM + nt * 16 + (lane & 15);
    unsigned short we[8];
#pragma unroll
    for (int jj = 0; jj < 8; ++jj) we[jj] = src[jj * DM];
    u32x4 v;
#pragma unroll
    for (int jj = 0; jj < 4; ++jj) v[jj] = (unsigned)we[2 * jj] | ((unsigned)we[2 * jj + 1] << 16);
    *(u32x4*)(ws + WS_WO + (size_t)f * 1024 + lane * 16) = v;
}

__global__ __launch_bounds__(512, 2) void qkv_kernel(const __hip_bfloat16* __restrict__ x, const __hip_bfloat16* __restrict__ wA,
                                                     const __hip_bfloat16* __restrict__ wB, const __hip_bfloat16* __restrict__ woA,
                                                     const __hip_bfloat16* __restrict__ woB, const __hip_bfloat16* __restrict__ gw,
                                                     char* __restrict__ ws) {
    extern __shared__ __attribute__((aligned(16))) char lds[];
    const int tid = threadIdx.x, lane = tid & 63;
    const int w = __builtin_amdgcn_readfirstlane(tid >> 6);
    const int bid = blockIdx.x;
    STAMP(0); RSTAMP(30);
    if (bid >= 240) { gate_job(x, gw, ws, bid - 240, w, lane); STAMP(1); RSTAMP(31); return; }
    const int t = (bid & 7) * 30 + (bid >> 3);
    const int tr = t % 120;
    const int n_tile = tr >> 3, m_tile = (t / 120) * 8 + (tr & 7);
    const int m0 = m_tile * 128, n0 = n_tile * 256;
    if (w >= 4) {
        const int lw = w - 4;
        const int row = 8 * lw + (lane >> 3);
        const int ch = (lane & 7) ^ ((row >> 1) & 7);
        const char* asrc = (const char*)(x + (size_t)(m0 + row) * DM + ch * 8);
        const int k = 2 * lw + (lane >> 5);
        const int s16 = lane & 31;
        const int c64 = (s16 >> 2) ^ (k & 3);
        const int col = n0 + c64 * 32 + (s16 & 3) * 8;
        const __hip_bfloat16* wp = (col < N3) ? (wA + col) : (wB + (col - N3));
        const char* bsrc = (const char*)(wp + (size_t)k * N3);
        gemm_stage(asrc, bsrc, (unsigned)(uintptr_t)lds, lw);
        const int f = t * 4 + lw;
        wo_job(woA, woB, ws, f, lane);
        if (f + 960 < 1600) wo_job(woA, woB, ws, f + 960, lane);
    } else {
        const int wm = w & 1, wn = w >> 1;
        const int ncol = n0 + wn * 128;
        const int br = ncol >= N3 ? 1 : 0;
        const int nb = ncol - br * N3;
        const int part = nb / DM;
        const int np = nb - part * DM;
        f32x16 acc[4][2];
#pragma unroll
        for (int i = 0; i < 4; ++i)
#pragma unroll
            for (int j = 0; j < 2; ++j) acc[i][j] = (f32x16){};
        const int r32 = lane & 31, h = lane >> 5;
        if (part == 2) {
            gemm_consume<true>(acc, lds, w, lane, ws);
            STAMP(2);
#pragma unroll
            for (int nt = 0; nt < 4; ++nt) {
                const int nn = np + nt * 32 + r32;
                const int head = nn / DH, d = nn - head * DH;
#pragma unroll
                for (int mt = 0; mt < 2; ++mt) {
                    const int tok0 = m0 + wm * 64 + mt * 32;
                    char* base = ws + WS_KV + (size_t)((br * 32 + head) * 8 + (tok0 >> 8)) * CHUNK + OFF_V + ((tok0 >> 5) & 7) * 1536 + h * 384;
#pragma unroll
                    for (int s = 0; s < 2; ++s) {
                        const f32x16& a = acc[nt][mt];
                        u32x4 v = {pack2(a[8 * s], a[8 * s + 1]), pack2(a[8 * s + 2], a[8 * s + 3]), pack2(a[8 * s + 4], a[8 * s + 5]),
                                   pack2(a[8 * s + 6], a[8 * s + 7])};
                        *(u32x4*)(base + s * 768 + d * 16) = v;
                        if (d == 0) *(u32x4*)(base + s * 768 + 320) = (u32x4){0x3f803f80u, 0x3f803f80u, 0x3f803f80u, 0x3f803f80u};
                    }
                }
            }
        } else {
            gemm_consume<false>(acc, lds, w, lane, ws);
            STAMP(2);
            const float sc = part == 0 ? QSCALE : 1.0f;
            const int tok = m0 + wm * 64 + r32;
            char* img = part == 0 ? ws + WS_Q + (size_t)br * 32 * QIMG + tok * 8
                                  : ws + WS_KV + (size_t)(br * 256 + (tok >> 8)) * CHUNK + (tok & 255) * 8;
            const int hstride = part == 0 ? QIMG : 8 * CHUNK, gstride = part == 0 ? 16384 : 2048;
#pragma unroll
            for (int nt = 0; nt < 4; ++nt) {
#pragma unroll
                for (int rg = 0; rg < 4; ++rg) {
                    const int nn = np + nt * 32 + 8 * rg + 4 * h;
                    const int head = nn / DH, gq = (nn - head * DH) >> 2;
                    char* dst = img + (size_t)head * hstride + gq * gstride;
#pragma unroll
                    for (int mt = 0; mt < 2; ++mt) {
                        const f32x16& a = acc[nt][mt];
                        u32x2 v = {pack2(a[4 * rg] * sc, a[4 * rg + 1] * sc), pack2(a[4 * rg + 2] * sc, a[4 * rg + 3] * sc)};
                        *(u32x2*)(dst + mt * 256) = v;
                    }
                }
            }
        }
        STAMP(3);
    }
    RSTAMP(31);
}
#undef DIAGK

constexpr int A_NSLOT = 3, A_LDS = 80 * 1024;
#define DIAGK 1

DI float half_swap_max(float m) {
    auto rr = __builtin_amdgcn_permlane32_swap(__float_as_uint(m), __float_as_uint(m), false, false);
    return __builtin_fmaxf(__uint_as_float(rr[0]), __uint_as_float(rr[1]));
}
DI float rowmax16(const f32x16& s) {
    float a = __builtin_fmaxf(__builtin_fmaxf(s[0], s[1]), s[2]);
#pragma unroll
    for (int r = 3; r < 15; r += 2) a = __builtin_fmaxf(__builtin_fmaxf(a, s[r]), s[r + 1]);
    a = __builtin_fmaxf(a, s[15]);
    return half_swap_max(a);
}
DI void softmax_pv(const f32x16& s, const bf16x8& v0, const bf16x8& v1, f32x16& o) {
    float p[16];
#pragma unroll
    for (int r = 0; r < 16; ++r) p[r] = __builtin_amdgcn_exp2f(s[r]);
    u32x4 p0 = {pack2(p[0], p[1]), pack2(p[2], p[3]), pack2(p[4], p[5]), pack2(p[6], p[7])};
    u32x4 p1 = {pack2(p[8], p[9]), pack2(p[10], p[11]), pack2(p[12], p[13]), pack2(p[14], p[15])};
    o = MFMA32(v0, __builtin_bit_cast(bf16x8, p0), o);
    o = MFMA32(v1, __builtin_bit_cast(bf16x8, p1), o);
}
DI int crow(int r, int hi) { return (r & 3) + 8 * (r >> 2) + 4 * hi; }

DI void attn_slow(f32x16& o, const char* kv, const bf16x8 q1, const bf16x4 q2, int ubeg, int uend, int mode, int qpos, int lane) {
    const int r32 = lane & 31, h = lane >> 5;
    const int vrow = r32 <= 20 ? r32 : 20;
    float m = -INFINITY;
    o = (f32x16){};
    for (int i = uend; i >= ubeg; --i) {
        const char* cb = kv + (size_t)(i >> 3) * CHUNK;
        const int t = i & 7;
        const char* kp = cb + (t * 32 + r32) * 8;
        bf16x8 k1 = cat8(*(const bf16x4*)(kp + h * 4096), *(const bf16x4*)(kp + h * 4096 + 2048));
        bf16x4 k2 = *(const bf16x4*)(kp + 8192);
        bf16x8 v0 = *(const bf16x8*)(cb + OFF_V + t * 1536 + h * 384 + vrow * 16);
        bf16x8 v1 = *(const bf16x8*)(cb + OFF_V + t * 1536 + 768 + h * 384 + vrow * 16);
        f32x16 s = (f32x16){};
        s = MFMA32(k1, q1, s);
        s = MFMA32K8(k2, q2, s);
        if (mode) {
#pragma unroll
            for (int r = 0; r < 16; ++r) {
                const int dlt = qpos - (i * 32 + crow(r, h));
                if (dlt < 0 || dlt >= 256) s[r] = -INFINITY;
            }
        }
        const float mn = __builtin_fmaxf(m, rowmax16(s));
        const float alpha = __builtin_amdgcn_exp2f(m - mn);
        m = mn;
#pragma unroll
        for (int r = 0; r < 16; ++r) { o[r] *= alpha; s[r] -= mn; }
        softmax_pv(s, v0, v1, o);
    }
}

DI void store_o(const f32x16& o, float l, char* oimg, int head, int qrow, int h) {
    const float rl = 1.0f / l;
    char* base = oimg + (size_t)(qrow >> 4) * 20480 + (head * 5 + h) * 128 + (qrow & 15) * 8;
#pragma unroll
    for (int rg = 0; rg < 3; ++rg) {
        if (8 * rg + 4 * h < DH) {
            const unsigned lo = pack2(o[4 * rg] * rl, o[4 * rg + 1] * rl), hi = pack2(o[4 * rg + 2] * rl, o[4 * rg + 3] * rl);
            *(u32x2*)(base + rg * 256) = (u32x2){lo, hi};
        }
    }
}
DI float get_l(const f32x16& o) {
    auto rr = __builtin_amdgcn_permlane32_swap(__float_as_uint(o[8]), __float_as_uint(o[8]), false, false);
    return __uint_as_float(rr[1]);
}
DI bool unsafe(const f32x16& o, float l) {
    float z = 0.f;
#pragma unroll
    for (int r = 0; r < 12; ++r) z = __builtin_fmaf(o[r], 0.f, z);
    return __any(!(l < LMAX) || !(z == 0.f));
}
#define EXP4(P, S, i) do { P[i] = __builtin_amdgcn_exp2f(S[i]); P[i + 1] = __builtin_amdgcn_exp2f(S[i + 1]); \
                           P[i + 2] = __builtin_amdgcn_exp2f(S[i + 2]); P[i + 3] = __builtin_amdgcn_exp2f(S[i + 3]); } while (0)
#define STEP(SC, SN, CN, Q1, Q2, KP, VP, VPN) do { \
    float p_[12]; \
    SN = MFMA32(cat8(k1a, k1b), Q1, CN); SBAR(); \
    k1a = lds_b64(KP); k1b = lds_b64((KP) + 2048); \
    EXP4(p_, SC, 0); pb[2] = pack2(pt[0], pt[1]); pb[3] = pack2(pt[2], pt[3]); SBAR(); \
    o = MFMA32(v1, __builtin_bit_cast(bf16x8, pb), o); SBAR(); \
    v1 = lds_b128((VP) + 768); \
    EXP4(p_, SC, 4); pa[0] = pack2(p_[0], p_[1]); pa[1] = pack2(p_[2], p_[3]); SBAR(); \
    SN = MFMA32K8(k2, Q2, SN); SBAR(); \
    k2 = lds_b64((KP) + k2d); \
    EXP4(p_, SC, 8); pa[2] = pack2(p_[4], p_[5]); pa[3] = pack2(p_[6], p_[7]); SBAR(); \
    o = MFMA32(v0, __builtin_bit_cast(bf16x8, pa), o); SBAR(); \
    v0 = lds_b128(VPN); \
    pt[0] = __builtin_amdgcn_exp2f(SC[12]); pt[1] = __builtin_amdgcn_exp2f(SC[13]); \
    pt[2] = __builtin_amdgcn_exp2f(SC[14]); pt[3] = __builtin_amdgcn_exp2f(SC[15]); \
    pb[0] = pack2(p_[8], p_[9]); pb[1] = pack2(p_[10], p_[11]); SBAR(); \
    } while (0)
#define DRAIN() do { pb[2] = pack2(pt[0], pt[1]); pb[3] = pack2(pt[2], pt[3]); o = MFMA32(v1, __builtin_bit_cast(bf16x8, pb), o); } while (0)

__global__ __launch_bounds__(512, 2) void attn_kernel(char* __restrict__ ws) {
    extern __shared__ __attribute__((aligned(16))) char lds[];
    const int tid = threadIdx.x, lane = tid & 63, r32 = lane & 31, h = lane >> 5;
    const int w = __builtin_amdgcn_readfirstlane(tid >> 6);
    const int bid = blockIdx.x;
    const int head = (bid & 7) * 4 + (bid >> 6), j = (bid >> 3) & 7;
    const unsigned lds0 = (unsigned)(uintptr_t)lds;
    const char* kvA = ws + WS_KV + (size_t)(head * 8) * CHUNK;
    const char* kvB = ws + WS_KV + (size_t)((32 + head) * 8) * CHUNK;
    const int qrow = j * 256 + w * 32 + r32;
    STAMP(0); RSTAMP(30);
    const int p3 = w < 6 ? 16384 : 0;
    auto issue = [&](int c, int sl) {
        const char* src = c < 8 ? kvA + (size_t)c * CHUNK : kvB + (size_t)(c == 8 ? j : (j > 0 ? j - 1 : 0)) * CHUNK;
        const unsigned dst = lds0 + sl * CHUNK + w * 1024;
        src += w * 1024 + lane * 16;
        glds16(src, (unsigned)__builtin_amdgcn_readfirstlane(dst));
        glds16(src + 8192, (unsigned)__builtin_amdgcn_readfirstlane(dst + 8192));
        glds16(src + p3, (unsigned)__builtin_amdgcn_readfirstlane(dst + p3));
    };
    const char* qp = ws + WS_Q + (size_t)head * QIMG + qrow * 8;
    bf16x4 qa, qb, q2;
    asm volatile("global_load_dwordx2 %0, %3, off\n\tglobal_load_dwordx2 %1, %4, off\n\tglobal_load_dwordx2 %2, %5, off"
                 : "=&v"(qa), "=&v"(qb), "=&v"(q2) : "v"(qp + h * 32768), "v"(qp + h * 32768 + 16384), "v"(qp + 65536) : "memory");
    issue(0, 0);
    issue(1, 1);
    asm volatile("s_waitcnt vmcnt(3) lgkmcnt(0)\n\ts_barrier" : "+v"(qa), "+v"(qb), "+v"(q2) :: "memory");
    STAMP(1);
    bf16x8 q1 = cat8(qa, qb);
    if (h) q2 = (bf16x4){};

    const int vrow = r32 <= 20 ? r32 : (r32 < 28 ? 20 : 19);
    const int koff = h * 4096 + r32 * 8, k2d = 8192 - h * 4096, voff = OFF_V + h * 384 + vrow * 16;

    f32x16 o = (f32x16){};
    f32x16 cc, sa, sb_;
    bf16x8 v0, v1;
    bf16x4 k1a, k1b, k2;
    u32x4 pa, pb = (u32x4){};
    float pt[4] = {0.f, 0.f, 0.f, 0.f};
    {
        lds_cptr kp = (lds_cptr)lds + koff;
        f32x16 s = (f32x16){};
        s = MFMA32(cat8(lds_b64(kp), lds_b64(kp + 2048)), q1, s);
        s = MFMA32K8(lds_b64(kp + k2d), q2, s);
        const float nm = -(rowmax16(s) + MBIAS);
#pragma unroll
        for (int r = 0; r < 16; ++r) { cc[r] = nm; sa[r] = s[r] + nm; }
        k1a = lds_b64(kp + 256); k1b = lds_b64(kp + 256 + 2048); k2 = lds_b64(kp + 256 + k2d);
        v0 = lds_b128((lds_cptr)lds + voff);
        v1 = v0;
    }
    int slot = 0;
#pragma unroll 1
    for (int c = 0; c < 8; ++c) {
        const int nslot = slot == 2 ? 0 : slot + 1, pslot = slot == 0 ? 2 : slot - 1;
        lds_cptr kc = (lds_cptr)lds + slot * CHUNK + koff, vc = (lds_cptr)lds + slot * CHUNK + voff;
        lds_cptr kn = (lds_cptr)lds + nslot * CHUNK + koff, vn = (lds_cptr)lds + nslot * CHUNK + voff;
        __builtin_amdgcn_s_setprio(2);
        STEP(sa, sb_, cc, q1, q2, kc + 2 * 256, vc + 0 * 1536, vc + 1 * 1536);
        STEP(sb_, sa, cc, q1, q2, kc + 3 * 256, vc + 1 * 1536, vc + 2 * 1536);
        __builtin_amdgcn_s_setprio(1);
        STEP(sa, sb_, cc, q1, q2, kc + 4 * 256, vc + 2 * 1536, vc + 3 * 1536);
        STEP(sb_, sa, cc, q1, q2, kc + 5 * 256, vc + 3 * 1536, vc + 4 * 1536);
        __builtin_amdgcn_s_setprio(0);
        STEP(sa, sb_, cc, q1, q2, kc + 6 * 256, vc + 4 * 1536, vc + 5 * 1536);
        STEP(sb_, sa, cc, q1, q2, kc + 7 * 256, vc + 5 * 1536, vc + 6 * 1536);
        WAIT_BAR(0);
        issue(c + 2, pslot);
        __builtin_amdgcn_s_setprio(3);
        STEP(sa, sb_, cc, q1, q2, kn + 0 * 256, vc + 6 * 1536, vc + 7 * 1536);
        STEP(sb_, sa, cc, q1, q2, kn + 1 * 256, vc + 7 * 1536, vn + 0 * 1536);
        slot = nslot;
    }
    DRAIN();
    STAMP(2);
    bf16x8 q1b;
    bf16x4 q2b;
    {
        const char* qpb = qp + (size_t)32 * QIMG;
        q1b = cat8(*(const bf16x4*)(qpb + h * 32768), *(const bf16x4*)(qpb + h * 32768 + 16384));
        q2b = *(const bf16x4*)(qpb + 65536);
    }
    {
        float l = get_l(o);
        if (__builtin_expect(unsafe(o, l), 0)) {
            attn_slow(o, kvA, q1, q2, 0, 63, 0, qrow, lane);
            l = get_l(o);
        }
        store_o(o, l, ws + WS_O, head, qrow, h);
    }
    if (h) q2b = (bf16x4){};
    STAMP(3);
    WAIT_BAR(0);
    STAMP(4);
    {
        constexpr int S1 = (8 % A_NSLOT) * CHUNK, S0 = (9 % A_NSLOT) * CHUNK;
        lds_cptr kb = (lds_cptr)lds + koff, vb = (lds_cptr)lds + voff;
        auto kad = [&](int u) { return kb + ((u < 8 ? S0 : S1) + (u & 7) * 256); };
        auto vad = [&](int u) { return vb + ((u < 8 ? S0 : S1) + (u & 7) * 1536); };
        o = (f32x16){};
        float nm;
        {
            f32x16 cm;
#pragma unroll
            for (int r = 0; r < 16; ++r) cm[r] = crow(r, h) <= r32 ? 0.f : -INFINITY;
            lds_cptr kp = kad(w + 8);
            f32x16 s = MFMA32(cat8(lds_b64(kp), lds_b64(kp + 2048)), q1b, cm);
            s = MFMA32K8(lds_b64(kp + k2d), q2b, s);
            nm = -(rowmax16(s) + MBIAS);
#pragma unroll
            for (int r = 0; r < 16; ++r) { sa[r] = s[r] + nm; cc[r] = nm; }
        }
        {
            pb = (u32x4){};
            pt[0] = pt[1] = pt[2] = pt[3] = 0.f;
            { lds_cptr kp = kad(w + 7); k1a = lds_b64(kp); k1b = lds_b64(kp + 2048); k2 = lds_b64(kp + k2d); }
            v0 = lds_b128(vad(w + 8));
            v1 = v0;
            const int kill = j == 0 ? w : -1;
            asm volatile("" : "+v"(cc));
#define KILL(i) do { if (kill == (i)) { _Pragma("unroll") for (int r = 0; r < 16; ++r) cc[r] = -INFINITY; asm volatile("" : "+v"(cc)); } } while (0)
            KILL(0); STEP(sa, sb_, cc, q1b, q2b, kad(w + 6), vad(w + 8), vad(w + 7));
            KILL(1); STEP(sb_, sa, cc, q1b, q2b, kad(w + 5), vad(w + 7), vad(w + 6));
            KILL(2); STEP(sa, sb_, cc, q1b, q2b, kad(w + 4), vad(w + 6), vad(w + 5));
            KILL(3); STEP(sb_, sa, cc, q1b, q2b, kad(w + 3), vad(w + 5), vad(w + 4));
            KILL(4); STEP(sa, sb_, cc, q1b, q2b, kad(w + 2), vad(w + 4), vad(w + 3));
            KILL(5); STEP(sb_, sa, cc, q1b, q2b, kad(w + 1), vad(w + 3), vad(w + 2));
            KILL(6); STEP(sa, sb_, cc, q1b, q2b, kad(w), vad(w + 2), vad(w + 1));
#pragma unroll
            for (int r = 0; r < 16; ++r) cc[r] = (j > 0 && crow(r, h) > r32) ? nm : -INFINITY;
            STEP(sb_, sa, cc, q1b, q2b, kad(w), vad(w + 1), vad(w));
            STEP(sa, sb_, cc, q1b, q2b, kad(w), vad(w), vad(w));
            DRAIN();
        }
        float l = get_l(o);
        if (__builtin_expect(unsafe(o, l), 0)) {
            const int ud = j * 8 + w;
            attn_slow(o, kvB, q1b, q2b, ud >= 8 ? ud - 8 : 0, ud, 1, qrow, lane);
            l = get_l(o);
        }
        store_o(o, l, ws + WS_O + WS_O_BYTES / 2, head, qrow, h);
    }
    STAMP(5); RSTAMP(31);
}
#undef DIAGK

constexpr int P_STAGE = 36 * 1024, P_LDS = 4 * P_STAGE;
#define DIAGK 2

__global__ __launch_bounds__(512, 2) void oproj_kernel(char* __restrict__ ws, __hip_bfloat16* __restrict__ out) {
    extern __shared__ __attribute__((aligned(16))) char lds[];
    const int tid = threadIdx.x, lane = tid & 63;
    const int w = __builtin_amdgcn_readfirstlane(tid >> 6);
    const int bid = blockIdx.x;
    const int mb = (bid & 7) * 4 + (bid >> 6), nb = (bid >> 3) & 7;
    const unsigned lds0 = (unsigned)(uintptr_t)lds;
    STAMP(0); RSTAMP(30);
    const char* src[5];
#pragma unroll
    for (int i = 0; i < 5; ++i) {
        int p = w + 8 * i;
        if (p >= 36) p = w;
        if (p < 16) {
            const int br = p >> 3, mt = (p >> 1) & 3, ks = p & 1;
            src[i] = ws + WS_O + ((size_t)(br * 128 + mb * 4 + mt) * 20 + ks) * 1024 + lane * 16;
        } else {
            const int pb = p - 16, br = pb / 10, nt = (pb % 10) >> 1, ks = pb & 1;
            src[i] = ws + WS_WO + ((size_t)(br * 40 + nb * 5 + nt) * 20 + ks) * 1024 + lane * 16;
        }
    }
    auto piece = [&](int s, int i) {
        if (i < 4 || w < 4) glds16(src[i] + s * 2048, (unsigned)__builtin_amdgcn_readfirstlane(lds0 + (s & 3) * P_STAGE + (w + 8 * i) * 1024));
    };
#pragma unroll
    for (int s = 0; s < 3; ++s)
#pragma unroll
        for (int i = 0; i < 5; ++i) piece(s, i);
    const int br = w >> 2, mh = (w >> 1) & 1, kp = w & 1;
    float g[2];
#pragma unroll
    for (int m = 0; m < 2; ++m) g[m] = ((const float*)(ws + WS_G))[mb * 64 + mh * 32 + m * 16 + (lane & 15)];
    f32x4 acc[2][5];
#pragma unroll
    for (int i = 0; i < 2; ++i)
#pragma unroll
        for (int n = 0; n < 5; ++n) acc[i][n] = (f32x4){};
    const int aoff = ((br * 4 + mh * 2) * 2 + kp) * 1024 + (lane >> 4) * 256 + (lane & 15) * 8;
    const int boff = (16 + br * 10 + kp) * 1024 + lane * 16;
    STAMP(1);
#pragma unroll
    for (int s = 0; s < 10; ++s) {
        if (s <= 7) { if (w < 4) WAIT_BAR(10); else WAIT_BAR(8); }
        else if (s == 8) { if (w < 4) WAIT_BAR(5); else WAIT_BAR(4); }
        else WAIT_BAR(0);
        lds_cptr sb = (lds_cptr)lds + (s & 3) * P_STAGE;
        bf16x8 af[2], bfr[5];
#pragma unroll
        for (int i = 0; i < 2; ++i) af[i] = cat8(lds_b64(sb + aoff + i * 2048), lds_b64(sb + aoff + i * 2048 + 128));
#pragma unroll
        for (int n = 0; n < 5; ++n) bfr[n] = lds_b128(sb + boff + n * 2048);
        if (s + 3 < 10) {
#pragma unroll
            for (int i = 0; i < 5; ++i) piece(s + 3, i);
        }
#pragma unroll
        for (int i = 0; i < 2; ++i)
#pragma unroll
            for (int n = 0; n < 5; ++n) acc[i][n] = MFMA16(bfr[n], af[i], acc[i][n]);
    }
    STAMP(2);
    WAIT_BAR(0);
    f32x4* part = (f32x4*)lds;
#pragma unroll
    for (int m = 0; m < 2; ++m)
#pragma unroll
        for (int n = 0; n < 5; ++n) part[(w * 10 + m * 5 + n) * 64 + lane] = acc[m][n];
    __syncthreads();
    const int qd = br * 2 + kp;
#pragma unroll
    for (int i = 0; i < 3; ++i) {
        const int tl = qd + 4 * i;
        if (tl < 10) {
            const int m = tl >= 5 ? 1 : 0, n = tl - 5 * m;
            const int row = mb * 64 + mh * 32 + m * 16 + (lane & 15);
            const float gg = m ? g[1] : g[0];
            const f32x4 a = part[((mh * 2 + 0) * 10 + tl) * 64 + lane] + part[((mh * 2 + 1) * 10 + tl) * 64 + lane];
            const f32x4 b = part[((4 + mh * 2 + 0) * 10 + tl) * 64 + lane] + part[((4 + mh * 2 + 1) * 10 + tl) * 64 + lane];
            const f32x4 v = gg * a + (1.0f - gg) * b;
            u32x2 pk = {pack2(v[0], v[1]), pack2(v[2], v[3])};
            *(u32x2*)(out + (size_t)row * DM + nb * 80 + n * 16 + (lane >> 4) * 4) = pk;
        }
    }
    STAMP(3); RSTAMP(31);
}
#undef DIAGK

extern "C" void kernel_launch(void* const* d_in, const int* in_sizes, int n_in, void* d_out, int out_size, void* d_ws, size_t ws_size,
                              hipStream_t stream) {
    const __hip_bfloat16* x = (const __hip_bfloat16*)d_in[0];
    const __hip_bfloat16* wA = (const __hip_bfloat16*)d_in[1];
    const __hip_bfloat16* woA = (const __hip_bfloat16*)d_in[2];
    const __hip_bfloat16* wB = (const __hip_bfloat16*)d_in[4];
    const __hip_bfloat16* woB = (const __hip_bfloat16*)d_in[5];
    const __hip_bfloat16* gw = (const __hip_bfloat16*)d_in[6];
    char* ws = (char*)d_ws;
    (void)hipFuncSetAttribute((const void*)qkv_kernel, hipFuncAttributeMaxDynamicSharedMemorySize, G_LDS);
    (void)hipFuncSetAttribute((const void*)attn_kernel, hipFuncAttributeMaxDynamicSharedMemorySize, A_LDS);
    (void)hipFuncSetAttribute((const void*)oproj_kernel, hipFuncAttributeMaxDynamicSharedMemorySize, P_LDS);
    qkv_kernel<<<256, 512, G_LDS, stream>>>(x, wA, wB, woA, woB, gw, ws);
    attn_kernel<<<256, 512, A_LDS, stream>>>(ws);
    oproj_kernel<<<256, 512, P_LDS, stream>>>(ws, (__hip_bfloat16*)d_out);
}
```

```cpp
#include <hip/hip_runtime.h>
#include <hip/hip_bf16.h>
#include <stdint.h>

typedef __attribute__((ext_vector_type(8))) short bf16x8;
typedef __attribute__((ext_vector_type(4))) short bf16x4;
typedef __attribute__((ext_vector_type(2))) __bf16 bf16v2;
typedef __attribute__((ext_vector_type(2))) float f32x2;
typedef __attribute__((ext_vector_type(16))) float f32x16;
typedef __attribute__((ext_vector_type(4))) float f32x4;
typedef __attribute__((ext_vector_type(4))) unsigned u32x4;
typedef __attribute__((ext_vector_type(2))) unsigned u32x2;
typedef __attribute__((address_space(3))) const char* lds_cptr;
typedef short v4i16_t __attribute__((ext_vector_type(4)));
typedef __attribute__((address_space(1))) unsigned gu32;
typedef __attribute__((address_space(1))) unsigned long long gu64;

#define DI __device__ __forceinline__
#define STAMP(i)
#define RSTAMP(i)
#define MFMA32(a, b, c) __builtin_amdgcn_mfma_f32_32x32x16_bf16((a), (b), (c), 0, 0, 0)
#define MFMA32K8(a, b, c) __builtin_amdgcn_mfma_f32_32x32x8bf16_1k((a), (b), (c), 0, 0, 0)
#define MFMA16(a, b, c) __builtin_amdgcn_mfma_f32_16x16x32_bf16((a), (b), (c), 0, 0, 0)
#define SBAR() __builtin_amdgcn_sched_barrier(0)

constexpr int SEQ = 2048, DM = 640, NH = 32, DH = 20, N3 = 1920;
constexpr int CHUNK = 22 * 1024;
constexpr int OFF_V = 10240;
constexpr size_t WS_KV = 0;
constexpr size_t WS_KV_BYTES = (size_t)2 * 32 * 8 * CHUNK;
constexpr int QIMG = 81920;
constexpr size_t WS_Q = WS_KV + WS_KV_BYTES;
constexpr size_t WS_Q_BYTES = (size_t)2 * 32 * QIMG;
constexpr size_t WS_O = WS_Q + WS_Q_BYTES;
constexpr size_t WS_O_BYTES = (size_t)2 * 128 * 20 * 1024;
constexpr size_t WS_WO = WS_O + WS_O_BYTES;
constexpr size_t WS_WO_BYTES = (size_t)2 * 40 * 20 * 1024;
constexpr size_t WS_G = WS_WO + WS_WO_BYTES;
constexpr size_t WS_CNT = WS_G + 8192;

constexpr float QSCALE = 0.22360679774997896f * 1.4426950408889634f;
constexpr float MBIAS = 96.0f;
constexpr float LMAX = 1.2676506e30f;

DI unsigned pack2(float lo, float hi) {
    f32x2 v = {lo, hi};
    bf16v2 b = __builtin_convertvector(v, bf16v2);
    return __builtin_bit_cast(unsigned, b);
}

DI void glds16(const void* g, unsigned lds_base) {
    unsigned sv;
    asm volatile("s_mov_b32 %0, m0\n\ts_mov_b32 m0, %2\n\ts_nop 0\n\tglobal_load_lds_dwordx4 %1, off\n\ts_mov_b32 m0, %0"
                 : "=&s"(sv) : "v"(g), "s"(lds_base) : "memory");
}
#define WAIT_BAR(N) asm volatile("s_waitcnt vmcnt(" #N ") lgkmcnt(0)\n\ts_barrier" ::: "memory")

DI bf16x8 lds_b128(lds_cptr p) { return *(const __attribute__((address_space(3))) bf16x8*)p; }
DI bf16x4 lds_b64(lds_cptr p) { return *(const __attribute__((address_space(3))) bf16x4*)p; }
DI bf16x4 lds_tr(lds_cptr p) {
    return __builtin_bit_cast(bf16x4, __builtin_amdgcn_ds_read_tr16_b64_v4i16((__attribute__((address_space(3))) v4i16_t*)p));
}
DI void gst8(char* p, u32x2 v) {
    __hip_atomic_store((gu64*)p, (unsigned long long)v[0] | ((unsigned long long)v[1] << 32), __ATOMIC_RELAXED, __HIP_MEMORY_SCOPE_AGENT);
}
DI void gst16(char* p, u32x4 v) { asm volatile("global_store_dwordx4 %0, %1, off sc1\n\ts_nop 1" :: "v"(p), "v"(v) : "memory"); }
DI bf16x8 cat8(bf16x4 lo, bf16x4 hi) { return __builtin_shufflevector(lo, hi, 0, 1, 2, 3, 4, 5, 6, 7); }

constexpr int G_STAGE = 48 * 1024, G_BOFF = 16 * 1024, G_LDS = 3 * G_STAGE;

template <bool ISV>
#define DIAGK 0
DI void gemm_consume(f32x16 (&acc)[4][2], char* lds, int w, int lane, char* ws) {
    const int r32 = lane & 31, h = lane >> 5;
    const int wm = w & 1, wn = w >> 1;
    const int arow = wm * 64 + r32;
    const int asw = (arow >> 1) & 7;
    int aoff[4];
#pragma unroll
    for (int kk = 0; kk < 4; ++kk) aoff[kk] = arow * 128 + (((2 * kk + h) ^ asw) << 4);
    const int g = lane >> 4, q = (lane & 15) >> 2, p = lane & 3;
    int boff[4];
#pragma unroll
    for (int nt = 0; nt < 4; ++nt) boff[nt] = G_BOFF + (8 * (g >> 1) + q) * 512 + (((4 * wn + nt) ^ q) << 6) + (16 * (g & 1) + 4 * p) * 2;
    bf16x8 a0[2], b0[4], a1[2], b1[4];
#define MM(A, B, nt, mt) do { if (ISV) acc[nt][mt] = MFMA32(A[mt], B[nt], acc[nt][mt]); else acc[nt][mt] = MFMA32(B[nt], A[mt], acc[nt][mt]); } while (0)
#define RB(BN, sb, kk, nt) BN[nt] = cat8(lds_tr(sb + boff[nt] + (kk) * 8192), lds_tr(sb + boff[nt] + (kk) * 8192 + 2048))
#define RA(AN, sb, kk) AN[0] = lds_b128(sb + aoff[kk]), AN[1] = lds_b128(sb + aoff[kk] + 4096)
#define GMMA(A, B, AN, BN, sb, kk) do { \
        MM(A, B, 0, 0); SBAR(); RA(AN, sb, kk); SBAR(); \
        MM(A, B, 0, 1); SBAR(); RB(BN, sb, kk, 0); SBAR(); \
        MM(A, B, 1, 0); SBAR(); RB(BN, sb, kk, 1); SBAR(); \
        MM(A, B, 1, 1); SBAR(); RB(BN, sb, kk, 2); SBAR(); \
        MM(A, B, 2, 0); SBAR(); RB(BN, sb, kk, 3); SBAR(); \
        MM(A, B, 2, 1); MM(A, B, 3, 0); MM(A, B, 3, 1); SBAR(); } while (0)
    WAIT_BAR(0);
    STAMP(1);
    {
        lds_cptr sb = (lds_cptr)lds;
        a0[0] = lds_b128(sb + aoff[0]); a0[1] = lds_b128(sb + aoff[0] + 4096);
#pragma unroll
        for (int nt = 0; nt < 4; ++nt) b0[nt] = cat8(lds_tr(sb + boff[nt]), lds_tr(sb + boff[nt] + 2048));
    }
    int sl = 0;
#pragma unroll 1
    for (int it = 0; it < 10; ++it) {
        const int nsl = sl == 2 ? 0 : sl + 1;
        lds_cptr sc = (lds_cptr)lds + sl * G_STAGE, sn = (lds_cptr)lds + nsl * G_STAGE;
        GMMA(a0, b0, a1, b1, sc, 1);
        GMMA(a1, b1, a0, b0, sc, 2);
        GMMA(a0, b0, a1, b1, sc, 3);
        if (it < 9) WAIT_BAR(0);
        GMMA(a1, b1, a0, b0, sn, 0);
        sl = nsl;
    }
#undef MM
#undef RB
#undef RA
#undef GMMA
}

DI void gemm_stage(const char* asrc, const char* bsrc, unsigned lds0, int lw) {
    auto issue = [&](int s, int sl) {
        const unsigned base = lds0 + sl * G_STAGE + lw * 1024;
#pragma unroll
        for (int i = 0; i < 4; ++i) glds16(asrc + s * 128 + i * (32 * DM * 2), (unsigned)__builtin_amdgcn_readfirstlane(base + i * 4096));
#pragma unroll
        for (int i = 0; i < 8; ++i) glds16(bsrc + (size_t)(s * 64 + i * 8) * N3 * 2, (unsigned)__builtin_amdgcn_readfirstlane(base + G_BOFF + i * 4096));
    };
    issue(0, 0);
    issue(1, 1);
    WAIT_BAR(12);
    issue(2, 2);
    int sl = 0;
#pragma unroll 1
    for (int it = 0; it < 9; ++it) {
        if (it <= 7) WAIT_BAR(12); else WAIT_BAR(0);
        if (it + 3 < 10) issue(it + 3, sl);
        sl = sl == 2 ? 0 : sl + 1;
    }
}

DI void gate_job(const __hip_bfloat16* __restrict__ x, const __hip_bfloat16* __restrict__ gw, char* __restrict__ ws, int sb, int w, int lane) {
    const unsigned short* xs = (const unsigned short*)x;
    const unsigned short* gs = (const unsigned short*)gw;
    float* gout = (float*)(ws + WS_G);
    unsigned gv[5];
#pragma unroll
    for (int i = 0; i < 5; ++i) gv[i] = *(const unsigned*)(gs + lane * 10 + 2 * i);
    float mine = 0.f;
#pragma unroll
    for (int r4 = 0; r4 < 16; r4 += 8) {
        unsigned xv[8][5];
#pragma unroll
        for (int rr = 0; rr < 8; ++rr)
#pragma unroll
            for (int i = 0; i < 5; ++i) xv[rr][i] = *(const unsigned*)(xs + (size_t)(sb * 128 + w * 16 + r4 + rr) * DM + lane * 10 + 2 * i);
#pragma unroll
        for (int rr = 0; rr < 8; ++rr) {
            float s = 0.f;
#pragma unroll
            for (int i = 0; i < 5; ++i) {
                s += __uint_as_float(xv[rr][i] << 16) * __uint_as_float(gv[i] << 16);
                s += __uint_as_float(xv[rr][i] & 0xffff0000u) * __uint_as_float(gv[i] & 0xffff0000u);
            }
#pragma unroll
            for (int o = 32; o > 0; o >>= 1) s += __shfl_xor(s, o);
            if (lane == r4 + rr) mine = s;
        }
    }
    if (lane < 16) gout[sb * 128 + w * 16 + lane] = 1.0f / (1.0f + __expf(-mine));
}
DI void wo_job(const __hip_bfloat16* __restrict__ woA, const __hip_bfloat16* __restrict__ woB, char* __restrict__ ws, int f, int lane) {
    const int br_ = f / 800, rem = f % 800, nt = rem / 20, ks = rem % 20;
    const unsigned short* src = (const unsigned short*)(br_ ? woB : woA) + (size_t)(ks * 32 + 8 * (lane >> 4)) * DM + nt * 16 + (lane & 15);
    unsigned short we[8];
#pragma unroll
    for (int jj = 0; jj < 8; ++jj) we[jj] = src[jj * DM];
    u32x4 v;
#pragma unroll
    for (int jj = 0; jj < 4; ++jj) v[jj] = (unsigned)we[2 * jj] | ((unsigned)we[2 * jj + 1] << 16);
    *(u32x4*)(ws + WS_WO + (size_t)f * 1024 + lane * 16) = v;
}

__global__ __launch_bounds__(512, 2) void qkv_kernel(const __hip_bfloat16* __restrict__ x, const __hip_bfloat16* __restrict__ wA,
                                                     const __hip_bfloat16* __restrict__ wB, const __hip_bfloat16* __restrict__ woA,
                                                     const __hip_bfloat16* __restrict__ woB, const __hip_bfloat16* __restrict__ gw,
                                                     char* __restrict__ ws) {
    extern __shared__ __attribute__((aligned(16))) char lds[];
    const int tid = threadIdx.x, lane = tid & 63;
    const int w = __builtin_amdgcn_readfirstlane(tid >> 6);
    const int bid = blockIdx.x;
    STAMP(0); RSTAMP(30);
    if (bid >= 240) { gate_job(x, gw, ws, bid - 240, w, lane); STAMP(1); RSTAMP(31); return; }
    const int t = (bid & 7) * 30 + (bid >> 3);
    const int tr = t % 120;
    const int n_tile = tr >> 3, m_tile = (t / 120) * 8 + (tr & 7);
    const int m0 = m_tile * 128, n0 = n_tile * 256;
    if (w >= 4) {
        const int lw = w - 4;
        const int row = 8 * lw + (lane >> 3);
        const int ch = (lane & 7) ^ ((row >> 1) & 7);
        const char* asrc = (const char*)(x + (size_t)(m0 + row) * DM + ch * 8);
        const int k = 2 * lw + (lane >> 5);
        const int s16 = lane & 31;
        const int c64 = (s16 >> 2) ^ (k & 3);
        const int col = n0 + c64 * 32 + (s16 & 3) * 8;
        const __hip_bfloat16* wp = (col < N3) ? (wA + col) : (wB + (col - N3));
        const char* bsrc = (const char*)(wp + (size_t)k * N3);
        gemm_stage(asrc, bsrc, (unsigned)(uintptr_t)lds, lw);
        const int f = t * 4 + lw;
        wo_job(woA, woB, ws, f, lane);
        if (f + 960 < 1600) wo_job(woA, woB, ws, f + 960, lane);
    } else {
        const int wm = w & 1, wn = w >> 1;
        const int ncol = n0 + wn * 128;
        const int br = ncol >= N3 ? 1 : 0;
        const int nb = ncol - br * N3;
        const int part = nb / DM;
        const int np = nb - part * DM;
        f32x16 acc[4][2];
#pragma unroll
        for (int i = 0; i < 4; ++i)
#pragma unroll
            for (int j = 0; j < 2; ++j) acc[i][j] = (f32x16){};
        const int r32 = lane & 31, h = lane >> 5;
        if (part == 2) {
            gemm_consume<true>(acc, lds, w, lane, ws);
            STAMP(2);
#pragma unroll
            for (int nt = 0; nt < 4; ++nt) {
                const int nn = np + nt * 32 + r32;
                const int head = nn / DH, d = nn - head * DH;
#pragma unroll
                for (int mt = 0; mt < 2; ++mt) {
                    const int tok0 = m0 + wm * 64 + mt * 32;
                    char* base = ws + WS_KV + (size_t)((br * 32 + head) * 8 + (tok0 >> 8)) * CHUNK + OFF_V + ((tok0 >> 5) & 7) * 1536 + h * 384;
#pragma unroll
                    for (int s = 0; s < 2; ++s) {
                        const f32x16& a = acc[nt][mt];
                        u32x4 v = {pack2(a[8 * s], a[8 * s + 1]), pack2(a[8 * s + 2], a[8 * s + 3]), pack2(a[8 * s + 4], a[8 * s + 5]),
                                   pack2(a[8 * s + 6], a[8 * s + 7])};
                        gst16(base + s * 768 + d * 16, v);
                        if (d == 0) gst16(base + s * 768 + 320, (u32x4){0x3f803f80u, 0x3f803f80u, 0x3f803f80u, 0x3f803f80u});
                    }
                }
            }
        } else {
            gemm_consume<false>(acc, lds, w, lane, ws);
            STAMP(2);
            const float sc = part == 0 ? QSCALE : 1.0f;
            const int tok = m0 + wm * 64 + r32;
            char* img = part == 0 ? ws + WS_Q + (size_t)br * 32 * QIMG + tok * 8
                                  : ws + WS_KV + (size_t)(br * 256 + (tok >> 8)) * CHUNK + (tok & 255) * 8;
            const int hstride = part == 0 ? QIMG : 8 * CHUNK, gstride = part == 0 ? 16384 : 2048;
#pragma unroll
            for (int nt = 0; nt < 4; ++nt) {
#pragma unroll
                for (int rg = 0; rg < 4; ++rg) {
                    const int nn = np + nt * 32 + 8 * rg + 4 * h;
                    const int head = nn / DH, gq = (nn - head * DH) >> 2;
                    char* dst = img + (size_t)head * hstride + gq * gstride;
#pragma unroll
                    for (int mt = 0; mt < 2; ++mt) {
                        const f32x16& a = acc[nt][mt];
                        u32x2 v = {pack2(a[4 * rg] * sc, a[4 * rg + 1] * sc), pack2(a[4 * rg + 2] * sc, a[4 * rg + 3] * sc)};
                        gst8(dst + mt * 256, v);
                    }
                }
            }
        }
        STAMP(3);
    }
    RSTAMP(31);
}
#undef DIAGK

constexpr int A_NSLOT = 4, A_CNT = A_NSLOT * CHUNK, A_LDS = A_CNT + 256;
#define DIAGK 1

DI float half_swap_max(float m) {
    auto rr = __builtin_amdgcn_permlane32_swap(__float_as_uint(m), __float_as_uint(m), false, false);
    return __builtin_fmaxf(__uint_as_float(rr[0]), __uint_as_float(rr[1]));
}
DI float rowmax16(const f32x16& s) {
    float a = __builtin_fmaxf(__builtin_fmaxf(s[0], s[1]), s[2]);
#pragma unroll
    for (int r = 3; r < 15; r += 2) a = __builtin_fmaxf(__builtin_fmaxf(a, s[r]), s[r + 1]);
    a = __builtin_fmaxf(a, s[15]);
    return half_swap_max(a);
}
DI void softmax_pv(const f32x16& s, const bf16x8& v0, const bf16x8& v1, f32x16& o) {
    float p[16];
#pragma unroll
    for (int r = 0; r < 16; ++r) p[r] = __builtin_amdgcn_exp2f(s[r]);
    u32x4 p0 = {pack2(p[0], p[1]), pack2(p[2], p[3]), pack2(p[4], p[5]), pack2(p[6], p[7])};
    u32x4 p1 = {pack2(p[8], p[9]), pack2(p[10], p[11]), pack2(p[12], p[13]), pack2(p[14], p[15])};
    o = MFMA32(v0, __builtin_bit_cast(bf16x8, p0), o);
    o = MFMA32(v1, __builtin_bit_cast(bf16x8, p1), o);
}
DI int crow(int r, int hi) { return (r & 3) + 8 * (r >> 2) + 4 * hi; }

DI void attn_slow(f32x16& o, const char* kv, const bf16x8 q1, const bf16x4 q2, int ubeg, int uend, int mode, int qpos, int lane) {
    const int r32 = lane & 31, h = lane >> 5;
    const int vrow = r32 <= 20 ? r32 : 20;
    float m = -INFINITY;
    o = (f32x16){};
    for (int i = uend; i >= ubeg; --i) {
        const char* cb = kv + (size_t)(i >> 3) * CHUNK;
        const int t = i & 7;
        const char* kp = cb + (t * 32 + r32) * 8;
        bf16x8 k1 = cat8(*(const bf16x4*)(kp + h * 4096), *(const bf16x4*)(kp + h * 4096 + 2048));
        bf16x4 k2 = *(const bf16x4*)(kp + 8192);
        bf16x8 v0 = *(const bf16x8*)(cb + OFF_V + t * 1536 + h * 384 + vrow * 16);
        bf16x8 v1 = *(const bf16x8*)(cb + OFF_V + t * 1536 + 768 + h * 384 + vrow * 16);
        f32x16 s = (f32x16){};
        s = MFMA32(k1, q1, s);
        s = MFMA32K8(k2, q2, s);
        if (mode) {
#pragma unroll
            for (int r = 0; r < 16; ++r) {
                const int dlt = qpos - (i * 32 + crow(r, h));
                if (dlt < 0 || dlt >= 256) s[r] = -INFINITY;
            }
        }
        const float mn = __builtin_fmaxf(m, rowmax16(s));
        const float alpha = __builtin_amdgcn_exp2f(m - mn);
        m = mn;
#pragma unroll
        for (int r = 0; r < 16; ++r) { o[r] *= alpha; s[r] -= mn; }
        softmax_pv(s, v0, v1, o);
    }
}

DI void store_o(const f32x16& o, float l, char* oimg, int head, int qrow, int h) {
    const float rl = 1.0f / l;
    char* base = oimg + (size_t)(qrow >> 4) * 20480 + (head * 5 + h) * 128 + (qrow & 15) * 8;
#pragma unroll
    for (int rg = 0; rg < 3; ++rg) {
        if (8 * rg + 4 * h < DH) {
            const unsigned lo = pack2(o[4 * rg] * rl, o[4 * rg + 1] * rl), hi = pack2(o[4 * rg + 2] * rl, o[4 * rg + 3] * rl);
            gst8(base + rg * 256, (u32x2){lo, hi});
        }
    }
}
DI float get_l(const f32x16& o) {
    auto rr = __builtin_amdgcn_permlane32_swap(__float_as_uint(o[8]), __float_as_uint(o[8]), false, false);
    return __uint_as_float(rr[1]);
}
DI bool unsafe(const f32x16& o, float l) {
    float z = 0.f;
#pragma unroll
    for (int r = 0; r < 12; ++r) z = __builtin_fmaf(o[r], 0.f, z);
    return __any(!(l < LMAX) || !(z == 0.f));
}
#define EXP4(P, S, i) do { P[i] = __builtin_amdgcn_exp2f(S[i]); P[i + 1] = __builtin_amdgcn_exp2f(S[i + 1]); \
                           P[i + 2] = __builtin_amdgcn_exp2f(S[i + 2]); P[i + 3] = __builtin_amdgcn_exp2f(S[i + 3]); } while (0)
#define STEP(SC, SN, CN, Q1, Q2, KP, VP, VPN) do { \
    float p_[12]; \
    SN = MFMA32(cat8(k1a, k1b), Q1, CN); SBAR(); \
    k1a = lds_b64(KP); k1b = lds_b64((KP) + 2048); \
    EXP4(p_, SC, 0); pb[2] = pack2(pt[0], pt[1]); pb[3] = pack2(pt[2], pt[3]); SBAR(); \
    o = MFMA32(v1, __builtin_bit_cast(bf16x8, pb), o); SBAR(); \
    v1 = lds_b128((VP) + 768); \
    EXP4(p_, SC, 4); pa[0] = pack2(p_[0], p_[1]); pa[1] = pack2(p_[2], p_[3]); SBAR(); \
    SN = MFMA32K8(k2, Q2, SN); SBAR(); \
    k2 = lds_b64((KP) + k2d); \
    EXP4(p_, SC, 8); pa[2] = pack2(p_[4], p_[5]); pa[3] = pack2(p_[6], p_[7]); SBAR(); \
    o = MFMA32(v0, __builtin_bit_cast(bf16x8, pa), o); SBAR(); \
    v0 = lds_b128(VPN); \
    pt[0] = __builtin_amdgcn_exp2f(SC[12]); pt[1] = __builtin_amdgcn_exp2f(SC[13]); \
    pt[2] = __builtin_amdgcn_exp2f(SC[14]); pt[3] = __builtin_amdgcn_exp2f(SC[15]); \
    pb[0] = pack2(p_[8], p_[9]); pb[1] = pack2(p_[10], p_[11]); \
    asm volatile("" : "+v"(pt[0]), "+v"(pt[1]), "+v"(pt[2]), "+v"(pt[3]), "+v"(pb)); SBAR();     \
    } while (0)
#define PRIO 1
#define SETPRIO(x) __builtin_amdgcn_s_setprio(x)
#define DRAIN() do { pb[2] = pack2(pt[0], pt[1]); pb[3] = pack2(pt[2], pt[3]); o = MFMA32(v1, __builtin_bit_cast(bf16x8, pb), o); } while (0)

#define ALB 2
__global__ __launch_bounds__(512, ALB) void attn_kernel(char* __restrict__ ws) {
    extern __shared__ __attribute__((aligned(16))) char lds[];
    const int tid = threadIdx.x, lane = tid & 63, r32 = lane & 31, h = lane >> 5;
    const int w = __builtin_amdgcn_readfirstlane(tid >> 6);
    const int bid = blockIdx.x;
    const int head = (bid & 7) * 4 + (bid >> 6), j = (bid >> 3) & 7;
    const unsigned lds0 = (unsigned)(uintptr_t)lds;
    const char* kvA = ws + WS_KV + (size_t)(head * 8) * CHUNK;
    const char* kvB = ws + WS_KV + (size_t)((32 + head) * 8) * CHUNK;
    const int qrow = j * 256 + w * 32 + r32;
    STAMP(0); RSTAMP(30);
    const int p3 = w < 6 ? 16384 : 0;
    auto issue = [&](int c, int sl) {
        const char* src = c < 8 ? kvA + (size_t)c * CHUNK : kvB + (size_t)(c == 8 ? j : (j > 0 ? j - 1 : 0)) * CHUNK;
        const unsigned dst = lds0 + sl * CHUNK + w * 1024;
        src += w * 1024 + lane * 16;
        glds16(src, (unsigned)__builtin_amdgcn_readfirstlane(dst));
        glds16(src + 8192, (unsigned)__builtin_amdgcn_readfirstlane(dst + 8192));
        glds16(src + p3, (unsigned)__builtin_amdgcn_readfirstlane(dst + p3));
    };
    const char* qp = ws + WS_Q + (size_t)head * QIMG + qrow * 8;
    bf16x4 qa, qb, q2;
    asm volatile("global_load_dwordx2 %0, %3, off\n\tglobal_load_dwordx2 %1, %4, off\n\tglobal_load_dwordx2 %2, %5, off"
                 : "=&v"(qa), "=&v"(qb), "=&v"(q2) : "v"(qp + h * 32768), "v"(qp + h * 32768 + 16384), "v"(qp + 65536) : "memory");
    issue(0, 0);
    issue(1, 1);
    if (tid < 32) *(volatile unsigned*)(lds + A_CNT + tid * 4) = 0u;
    asm volatile("s_waitcnt vmcnt(3) lgkmcnt(0)\n\ts_barrier" : "+v"(qa), "+v"(qb), "+v"(q2) :: "memory");
    STAMP(1);
    issue(2, 2);
    bf16x8 q1 = cat8(qa, qb);
    if (h) q2 = (bf16x4){};
    bf16x8 q1b;
    bf16x4 q2b;
    {
        const char* qpb = qp + (size_t)32 * QIMG;
        q1b = cat8(*(const bf16x4*)(qpb + h * 32768), *(const bf16x4*)(qpb + h * 32768 + 16384));
        q2b = *(const bf16x4*)(qpb + 65536);
    }

    const int vrow = r32 <= 20 ? r32 : (r32 < 28 ? 20 : 19);
    const int koff = h * 4096 + r32 * 8, k2d = 8192 - h * 4096, voff = OFF_V + h * 384 + vrow * 16;
    constexpr int S1 = (8 % A_NSLOT) * CHUNK, S0 = (9 % A_NSLOT) * CHUNK;
    lds_cptr kb = (lds_cptr)lds + koff, vb = (lds_cptr)lds + voff;
    auto kad = [&](int u) { return kb + ((u < 8 ? S0 : S1) + (u & 7) * 256); };
    auto vad = [&](int u) { return vb + ((u < 8 ? S0 : S1) + (u & 7) * 1536); };

    f32x16 o = (f32x16){};
    f32x16 cc, sa, sb_;
    bf16x8 v0, v1;
    bf16x4 k1a, k1b, k2;
    u32x4 pa, pb = (u32x4){};
    float pt[4] = {0.f, 0.f, 0.f, 0.f};
    {
        lds_cptr kp = kb;
        f32x16 s = (f32x16){};
        s = MFMA32(cat8(lds_b64(kp), lds_b64(kp + 2048)), q1, s);
        s = MFMA32K8(lds_b64(kp + k2d), q2, s);
        const float nm = -(rowmax16(s) + MBIAS);
#pragma unroll
        for (int r = 0; r < 16; ++r) { cc[r] = nm; sa[r] = s[r] + nm; }
        k1a = lds_b64(kp + 256); k1b = lds_b64(kp + 256 + 2048); k2 = lds_b64(kp + 256 + k2d);
        v0 = lds_b128(vb);
        v1 = v0;
    }
    asm volatile("" : "+v"(cc));
#pragma unroll 1
    for (int c = 0; c < 8; ++c) {
        const int slot = c & 3, nslot = (c + 1) & 3;
        lds_cptr kc = kb + slot * CHUNK, vc = vb + slot * CHUNK;
        lds_cptr kn0 = c < 7 ? kb + nslot * CHUNK : kad(w + 8), kn1 = c < 7 ? kb + nslot * CHUNK + 256 : kad(w + 7);
        lds_cptr vn0 = c < 7 ? vb + nslot * CHUNK : vad(w + 8);
        typedef __attribute__((address_space(3))) volatile unsigned* lds_vu;
        const unsigned cL = lds0 + A_CNT + (c + 1) * 4, cD = lds0 + A_CNT + 64 + c * 4;
#define SIGNAL(addr) asm volatile("s_mov_b64 exec, 1\n\tds_add_u32 %0, %1\n\ts_mov_b64 exec, -1" :: "v"(addr), "v"(1u) : "memory")
#define AWAIT(val, addr) do { if (__builtin_expect((unsigned)__builtin_amdgcn_readfirstlane(val) < 8u, 0)) { \
            while ((unsigned)__builtin_amdgcn_readfirstlane(*(lds_vu)(uintptr_t)(addr)) < 8u) __builtin_amdgcn_s_sleep(1); } \
            asm volatile("" ::: "memory"); } while (0)
        SETPRIO(2);
        STEP(sa, sb_, cc, q1, q2, kc + 2 * 256, vc + 0 * 1536, vc + 1 * 1536);
        SIGNAL(cD);
        STEP(sb_, sa, cc, q1, q2, kc + 3 * 256, vc + 1 * 1536, vc + 2 * 1536);
        if (c < 7) asm volatile("s_waitcnt vmcnt(3)" ::: "memory"); else asm volatile("s_waitcnt vmcnt(0)" ::: "memory");
        SIGNAL(cL);
        SETPRIO(1);
        STEP(sa, sb_, cc, q1, q2, kc + 4 * 256, vc + 2 * 1536, vc + 3 * 1536);
        STEP(sb_, sa, cc, q1, q2, kc + 5 * 256, vc + 3 * 1536, vc + 4 * 1536);
        const unsigned vD = *(lds_vu)(uintptr_t)cD;
        SETPRIO(0);
        STEP(sa, sb_, cc, q1, q2, kc + 6 * 256, vc + 4 * 1536, vc + 5 * 1536);
        const unsigned vL = *(lds_vu)(uintptr_t)cL;
        if (c + 3 < 10) {
            AWAIT(vD, cD);
            issue(c + 3, (c + 3) & 3);
        }
        STEP(sb_, sa, cc, q1, q2, kc + 7 * 256, vc + 5 * 1536, vc + 6 * 1536);
        AWAIT(vL, cL);
        SETPRIO(3);
        STEP(sa, sb_, cc, q1, q2, kn0, vc + 6 * 1536, vc + 7 * 1536);
        if (c == 7) {
            q1 = q1b;
            q2 = h ? (bf16x4){} : q2b;
#pragma unroll
            for (int r = 0; r < 16; ++r) cc[r] = crow(r, h) <= r32 ? 0.f : -INFINITY;
            asm volatile("" : "+v"(cc), "+v"(q1), "+v"(q2));
        }
        STEP(sb_, sa, cc, q1, q2, kn1, vc + 7 * 1536, vn0);
    }
    DRAIN();
    STAMP(2);
    {
        float l = get_l(o);
        if (__builtin_expect(unsafe(o, l), 0)) {
            bf16x8 q1s = cat8(*(const bf16x4*)(qp + h * 32768), *(const bf16x4*)(qp + h * 32768 + 16384));
            bf16x4 q2s = *(const bf16x4*)(qp + 65536);
            if (h) q2s = (bf16x4){};
            attn_slow(o, kvA, q1s, q2s, 0, 63, 0, qrow, lane);
            l = get_l(o);
        }
        store_o(o, l, ws + WS_O, head, qrow, h);
    }
    STAMP(3);
    STAMP(4);
    {
        o = (f32x16){};
        const float nm = -(rowmax16(sa) + MBIAS);
#pragma unroll
        for (int r = 0; r < 16; ++r) { sa[r] += nm; cc[r] = nm; }
        {
            pb = (u32x4){};
            pt[0] = pt[1] = pt[2] = pt[3] = 0.f;
            const int kill = j == 0 ? w : -1;
            asm volatile("" : "+v"(cc));
#define KILL(i) do { if (kill == (i)) { _Pragma("unroll") for (int r = 0; r < 16; ++r) cc[r] = -INFINITY; asm volatile("" : "+v"(cc)); } } while (0)
            KILL(0); STEP(sa, sb_, cc, q1, q2, kad(w + 6), vad(w + 8), vad(w + 7));
            KILL(1); STEP(sb_, sa, cc, q1, q2, kad(w + 5), vad(w + 7), vad(w + 6));
            KILL(2); STEP(sa, sb_, cc, q1, q2, kad(w + 4), vad(w + 6), vad(w + 5));
            KILL(3); STEP(sb_, sa, cc, q1, q2, kad(w + 3), vad(w + 5), vad(w + 4));
            KILL(4); STEP(sa, sb_, cc, q1, q2, kad(w + 2), vad(w + 4), vad(w + 3));
            KILL(5); STEP(sb_, sa, cc, q1, q2, kad(w + 1), vad(w + 3), vad(w + 2));
            KILL(6); STEP(sa, sb_, cc, q1, q2, kad(w), vad(w + 2), vad(w + 1));
#pragma unroll
            for (int r = 0; r < 16; ++r) cc[r] = (j > 0 && crow(r, h) > r32) ? nm : -INFINITY;
            STEP(sb_, sa, cc, q1, q2, kad(w), vad(w + 1), vad(w));
            STEP(sa, sb_, cc, q1, q2, kad(w), vad(w), vad(w));
            DRAIN();
        }
        float l = get_l(o);
        if (__builtin_expect(unsafe(o, l), 0)) {
            const int ud = j * 8 + w;
            attn_slow(o, kvB, q1, q2, ud >= 8 ? ud - 8 : 0, ud, 1, qrow, lane);
            l = get_l(o);
        }
        store_o(o, l, ws + WS_O + WS_O_BYTES / 2, head, qrow, h);
    }
    STAMP(5); RSTAMP(31);
}
#undef DIAGK

constexpr int P_STAGE = 36 * 1024, P_LDS = 4 * P_STAGE;
#define DIAGK 2

__global__ __launch_bounds__(512, 2) void oproj_kernel(char* __restrict__ ws, __hip_bfloat16* __restrict__ out) {
    extern __shared__ __attribute__((aligned(16))) char lds[];
    const int tid = threadIdx.x, lane = tid & 63;
    const int w = __builtin_amdgcn_readfirstlane(tid >> 6);
    const int bid = blockIdx.x;
    const int mb = (bid & 7) * 4 + (bid >> 6), nb = (bid >> 3) & 7;
    const unsigned lds0 = (unsigned)(uintptr_t)lds;
    STAMP(0); RSTAMP(30);
    const char* src[5];
#pragma unroll
    for (int i = 0; i < 5; ++i) {
        int p = w + 8 * i;
        if (p >= 36) p = w;
        if (p < 16) {
            const int br = p >> 3, mt = (p >> 1) & 3, ks = p & 1;
            src[i] = ws + WS_O + ((size_t)(br * 128 + mb * 4 + mt) * 20 + ks) * 1024 + lane * 16;
        } else {
            const int pb = p - 16, br = pb / 10, nt = (pb % 10) >> 1, ks = pb & 1;
            src[i] = ws + WS_WO + ((size_t)(br * 40 + nb * 5 + nt) * 20 + ks) * 1024 + lane * 16;
        }
    }
    auto piece = [&](int s, int i) {
        if (i < 4 || w < 4) glds16(src[i] + s * 2048, (unsigned)__builtin_amdgcn_readfirstlane(lds0 + (s & 3) * P_STAGE + (w + 8 * i) * 1024));
    };
#pragma unroll
    for (int s = 0; s < 3; ++s)
#pragma unroll
        for (int i = 0; i < 5; ++i) piece(s, i);
    const int br = w >> 2, mh = (w >> 1) & 1, kp = w & 1;
    float g[2];
#pragma unroll
    for (int m = 0; m < 2; ++m) g[m] = ((const float*)(ws + WS_G))[mb * 64 + mh * 32 + m * 16 + (lane & 15)];
    f32x4 acc[2][5];
#pragma unroll
    for (int i = 0; i < 2; ++i)
#pragma unroll
        for (int n = 0; n < 5; ++n) acc[i][n] = (f32x4){};
    const int aoff = ((br * 4 + mh * 2) * 2 + kp) * 1024 + (lane >> 4) * 256 + (lane & 15) * 8;
    const int boff = (16 + br * 10 + kp) * 1024 + lane * 16;
    STAMP(1);
#pragma unroll
    for (int s = 0; s < 10; ++s) {
        if (s <= 7) { if (w < 4) WAIT_BAR(10); else WAIT_BAR(8); }
        else if (s == 8) { if (w < 4) WAIT_BAR(5); else WAIT_BAR(4); }
        else WAIT_BAR(0);
        lds_cptr sb = (lds_cptr)lds + (s & 3) * P_STAGE;
        bf16x8 af[2], bfr[5];
#pragma unroll
        for (int i = 0; i < 2; ++i) af[i] = cat8(lds_b64(sb + aoff + i * 2048), lds_b64(sb + aoff + i * 2048 + 128));
#pragma unroll
        for (int n = 0; n < 5; ++n) bfr[n] = lds_b128(sb + boff + n * 2048);
        if (s + 3 < 10) {
#pragma unroll
            for (int i = 0; i < 5; ++i) piece(s + 3, i);
        }
#pragma unroll
        for (int i = 0; i < 2; ++i)
#pragma unroll
            for (int n = 0; n < 5; ++n) acc[i][n] = MFMA16(bfr[n], af[i], acc[i][n]);
    }
    STAMP(2);
    WAIT_BAR(0);
    f32x4* part = (f32x4*)lds;
#pragma unroll
    for (int m = 0; m < 2; ++m)
#pragma unroll
        for (int n = 0; n < 5; ++n) part[(w * 10 + m * 5 + n) * 64 + lane] = acc[m][n];
    __syncthreads();
    const int qd = br * 2 + kp;
#pragma unroll
    for (int i = 0; i < 3; ++i) {
        const int tl = qd + 4 * i;
        if (tl < 10) {
            const int m = tl >= 5 ? 1 : 0, n = tl - 5 * m;
            const int row = mb * 64 + mh * 32 + m * 16 + (lane & 15);
            const float gg = m ? g[1] : g[0];
            const f32x4 a = part[((mh * 2 + 0) * 10 + tl) * 64 + lane] + part[((mh * 2 + 1) * 10 + tl) * 64 + lane];
            const f32x4 b = part[((4 + mh * 2 + 0) * 10 + tl) * 64 + lane] + part[((4 + mh * 2 + 1) * 10 + tl) * 64 + lane];
            const f32x4 v = gg * a + (1.0f - gg) * b;
            u32x2 pk = {pack2(v[0], v[1]), pack2(v[2], v[3])};
            *(u32x2*)(out + (size_t)row * DM + nb * 80 + n * 16 + (lane >> 4) * 4) = pk;
        }
    }
    STAMP(3); RSTAMP(31);
}
#undef DIAGK

extern "C" void kernel_launch(void* const* d_in, const int* in_sizes, int n_in, void* d_out, int out_size, void* d_ws, size_t ws_size,
                              hipStream_t stream) {
    const __hip_bfloat16* x = (const __hip_bfloat16*)d_in[0];
    const __hip_bfloat16* wA = (const __hip_bfloat16*)d_in[1];
    const __hip_bfloat16* woA = (const __hip_bfloat16*)d_in[2];
    const __hip_bfloat16* wB = (const __hip_bfloat16*)d_in[4];
    const __hip_bfloat16* woB = (const __hip_bfloat16*)d_in[5];
    const __hip_bfloat16* gw = (const __hip_bfloat16*)d_in[6];
    char* ws = (char*)d_ws;
    (void)hipFuncSetAttribute((const void*)qkv_kernel, hipFuncAttributeMaxDynamicSharedMemorySize, G_LDS);
    (void)hipFuncSetAttribute((const void*)attn_kernel, hipFuncAttributeMaxDynamicSharedMemorySize, A_LDS);
    (void)hipFuncSetAttribute((const void*)oproj_kernel, hipFuncAttributeMaxDynamicSharedMemorySize, P_LDS);
    qkv_kernel<<<256, 512, G_LDS, stream>>>(x, wA, wB, woA, woB, gw, ws);
    attn_kernel<<<256, 512, A_LDS, stream>>>(ws);
    oproj_kernel<<<256, 512, P_LDS, stream>>>(ws, (__hip_bfloat16*)d_out);
}
```

```cpp
#include <hip/hip_runtime.h>
#include <hip/hip_bf16.h>
#include <stdint.h>

typedef __attribute__((ext_vector_type(8))) short bf16x8;
typedef __attribute__((ext_vector_type(4))) short bf16x4;
typedef __attribute__((ext_vector_type(2))) __bf16 bf16v2;
typedef __attribute__((ext_vector_type(2))) float f32x2;
typedef __attribute__((ext_vector_type(16))) float f32x16;
typedef __attribute__((ext_vector_type(4))) float f32x4;
typedef __attribute__((ext_vector_type(4))) unsigned u32x4;
typedef __attribute__((ext_vector_type(2))) unsigned u32x2;
typedef __attribute__((address_space(3))) const char* lds_cptr;
typedef short v4i16_t __attribute__((ext_vector_type(4)));
typedef __attribute__((address_space(1))) unsigned gu32;
typedef __attribute__((address_space(1))) unsigned long long gu64;

#define DI __device__ __forceinline__
#define MFMA32(a, b, c) __builtin_amdgcn_mfma_f32_32x32x16_bf16((a), (b), (c), 0, 0, 0)
#define MFMA32K8(a, b, c) __builtin_amdgcn_mfma_f32_32x32x8bf16_1k((a), (b), (c), 0, 0, 0)
#define MFMA16(a, b, c) __builtin_amdgcn_mfma_f32_16x16x32_bf16((a), (b), (c), 0, 0, 0)
#define SBAR() __builtin_amdgcn_sched_barrier(0)

constexpr int SEQ = 2048, DM = 640, NH = 32, DH = 20, N3 = 1920;
constexpr int CHUNK = 22 * 1024;
constexpr int OFF_V = 10240;
constexpr size_t WS_KV = 0;
constexpr size_t WS_KV_BYTES = (size_t)2 * 32 * 8 * CHUNK;
constexpr int QIMG = 81920;
constexpr size_t WS_Q = WS_KV + WS_KV_BYTES;
constexpr size_t WS_Q_BYTES = (size_t)2 * 32 * QIMG;
constexpr size_t WS_O = WS_Q + WS_Q_BYTES;
constexpr size_t WS_O_BYTES = (size_t)2 * 128 * 20 * 1024;
constexpr size_t WS_WO = WS_O + WS_O_BYTES;
constexpr size_t WS_WO_BYTES = (size_t)2 * 40 * 20 * 1024;
constexpr size_t WS_G = WS_WO + WS_WO_BYTES;

constexpr float QSCALE = 0.22360679774997896f * 1.4426950408889634f;
constexpr float MBIAS = 96.0f;
constexpr float LMAX = 1.3292280e36f;
constexpr float OMIN = 7.5231638e-37f;

DI unsigned pack2(float lo, float hi) {
    f32x2 v = {lo, hi};
    bf16v2 b = __builtin_convertvector(v, bf16v2);
    return __builtin_bit_cast(unsigned, b);
}

DI void glds16(const void* g, unsigned lds_base) {
    unsigned sv;
    asm volatile("s_mov_b32 %0, m0\n\ts_mov_b32 m0, %2\n\ts_nop 0\n\tglobal_load_lds_dwordx4 %1, off\n\ts_mov_b32 m0, %0"
                 : "=&s"(sv) : "v"(g), "s"(lds_base) : "memory");
}
#define WAIT_BAR(N) asm volatile("s_waitcnt vmcnt(" #N ") lgkmcnt(0)\n\ts_barrier" ::: "memory")
#define LGKM_BAR() asm volatile("s_waitcnt lgkmcnt(0)\n\ts_barrier" ::: "memory")

DI bf16x8 lds_b128(lds_cptr p) { return *(const __attribute__((address_space(3))) bf16x8*)p; }
DI bf16x4 lds_b64(lds_cptr p) { return *(const __attribute__((address_space(3))) bf16x4*)p; }
DI bf16x4 lds_tr(lds_cptr p) {
    return __builtin_bit_cast(bf16x4, __builtin_amdgcn_ds_read_tr16_b64_v4i16((__attribute__((address_space(3))) v4i16_t*)p));
}
DI void gst8(char* p, u32x2 v) {
    __hip_atomic_store((gu64*)p, (unsigned long long)v[0] | ((unsigned long long)v[1] << 32), __ATOMIC_RELAXED, __HIP_MEMORY_SCOPE_AGENT);
}
DI void gst16(char* p, u32x4 v) { asm volatile("global_store_dwordx4 %0, %1, off sc1\n\ts_nop 1" :: "v"(p), "v"(v) : "memory"); }
DI void gst_pair(char* pa, char* pb, u32x2 a, u32x2 b, int lane) {
    const bool odd = lane & 1;
    const unsigned g0 = odd ? a[0] : b[0], g1 = odd ? a[1] : b[1];
    const unsigned y0 = __builtin_amdgcn_mov_dpp(g0, 0xB1, 0xF, 0xF, true), y1 = __builtin_amdgcn_mov_dpp(g1, 0xB1, 0xF, 0xF, true);
    const u32x4 v = odd ? (u32x4){y0, y1, b[0], b[1]} : (u32x4){a[0], a[1], y0, y1};
    gst16(odd ? pb - 8 : pa, v);
}
DI bf16x8 cat8(bf16x4 lo, bf16x4 hi) { return __builtin_shufflevector(lo, hi, 0, 1, 2, 3, 4, 5, 6, 7); }

constexpr int G_STAGE = 48 * 1024, G_BOFF = 16 * 1024, G_LDS = 3 * G_STAGE;

template <bool ISV>
DI void gemm_consume(f32x16 (&acc)[4][2], char* lds, int w, int lane) {
    const int r32 = lane & 31, h = lane >> 5;
    const int wm = w & 1, wn = w >> 1;
    const int arow = wm * 64 + r32;
    const int asw = (arow >> 1) & 7;
    int aoff[4];
#pragma unroll
    for (int kk = 0; kk < 4; ++kk) aoff[kk] = arow * 128 + (((2 * kk + h) ^ asw) << 4);
    const int g = lane >> 4, q = (lane & 15) >> 2, p = lane & 3;
    int boff[4];
#pragma unroll
    for (int nt = 0; nt < 4; ++nt) boff[nt] = G_BOFF + (8 * (g >> 1) + q) * 512 + (((4 * wn + nt) ^ q) << 6) + (16 * (g & 1) + 4 * p) * 2;
    bf16x8 a0[2], b0[4], a1[2], b1[4];
#define MM(A, B, nt, mt) do { if (ISV) acc[nt][mt] = MFMA32(A[mt], B[nt], acc[nt][mt]); else acc[nt][mt] = MFMA32(B[nt], A[mt], acc[nt][mt]); } while (0)
#define RB(BN, sb, kk, nt) BN[nt] = cat8(lds_tr(sb + boff[nt] + (kk) * 8192), lds_tr(sb + boff[nt] + (kk) * 8192 + 2048))
#define RA(AN, sb, kk) AN[0] = lds_b128(sb + aoff[kk]), AN[1] = lds_b128(sb + aoff[kk] + 4096)
#define GMMA(A, B, AN, BN, sb, kk) do { \
        MM(A, B, 0, 0); SBAR(); RA(AN, sb, kk); SBAR(); \
        MM(A, B, 0, 1); SBAR(); RB(BN, sb, kk, 0); SBAR(); \
        MM(A, B, 1, 0); SBAR(); RB(BN, sb, kk, 1); SBAR(); \
        MM(A, B, 1, 1); SBAR(); RB(BN, sb, kk, 2); SBAR(); \
        MM(A, B, 2, 0); SBAR(); RB(BN, sb, kk, 3); SBAR(); \
        MM(A, B, 2, 1); MM(A, B, 3, 0); MM(A, B, 3, 1); SBAR(); } while (0)
    LGKM_BAR();

    {
        lds_cptr sb = (lds_cptr)lds;
        a0[0] = lds_b128(sb + aoff[0]); a0[1] = lds_b128(sb + aoff[0] + 4096);
#pragma unroll
        for (int nt = 0; nt < 4; ++nt) b0[nt] = cat8(lds_tr(sb + boff[nt]), lds_tr(sb + boff[nt] + 2048));
    }
    int sl = 0;
#pragma unroll 1
    for (int it = 0; it < 10; ++it) {
        const int nsl = sl == 2 ? 0 : sl + 1;
        lds_cptr sc = (lds_cptr)lds + sl * G_STAGE, sn = (lds_cptr)lds + nsl * G_STAGE;
        GMMA(a0, b0, a1, b1, sc, 1);
        GMMA(a1, b1, a0, b0, sc, 2);
        GMMA(a0, b0, a1, b1, sc, 3);
        if (it < 9) LGKM_BAR();
        GMMA(a1, b1, a0, b0, sn, 0);
        sl = nsl;
    }
#undef MM
#undef RB
#undef RA
#undef GMMA
}

DI void gemm_stage(const char* asrc, const char* bsrc, unsigned lds0, int lw) {
    auto issue = [&](int s, int sl) {
        const unsigned base = lds0 + sl * G_STAGE + lw * 1024;
#pragma unroll
        for (int i = 0; i < 4; ++i) glds16(asrc + s * 128 + i * (32 * DM * 2), (unsigned)__builtin_amdgcn_readfirstlane(base + i * 4096));
#pragma unroll
        for (int i = 0; i < 8; ++i) glds16(bsrc + (size_t)(s * 64 + i * 8) * N3 * 2, (unsigned)__builtin_amdgcn_readfirstlane(base + G_BOFF + i * 4096));
    };
    issue(0, 0);
    issue(1, 1);
    WAIT_BAR(12);
    issue(2, 2);
    int sl = 0;
#pragma unroll 1
    for (int it = 0; it < 9; ++it) {
        if (it <= 7) WAIT_BAR(12); else WAIT_BAR(0);
        if (it + 3 < 10) issue(it + 3, sl);
        sl = sl == 2 ? 0 : sl + 1;
    }
}

DI void gate_job(const __hip_bfloat16* __restrict__ x, const __hip_bfloat16* __restrict__ gw, char* __restrict__ ws, int sb, int w, int lane) {
    const unsigned short* xs = (const unsigned short*)x;
    const unsigned short* gs = (const unsigned short*)gw;
    float* gout = (float*)(ws + WS_G);
    unsigned gv[5];
#pragma unroll
    for (int i = 0; i < 5; ++i) gv[i] = *(const unsigned*)(gs + lane * 10 + 2 * i);
    float mine = 0.f;
#pragma unroll
    for (int r4 = 0; r4 < 16; r4 += 8) {
        unsigned xv[8][5];
#pragma unroll
        for (int rr = 0; rr < 8; ++rr)
#pragma unroll
            for (int i = 0; i < 5; ++i) xv[rr][i] = *(const unsigned*)(xs + (size_t)(sb * 128 + w * 16 + r4 + rr) * DM + lane * 10 + 2 * i);
#pragma unroll
        for (int rr = 0; rr < 8; ++rr) {
            float s = 0.f;
#pragma unroll
            for (int i = 0; i < 5; ++i) {
                s += __uint_as_float(xv[rr][i] << 16) * __uint_as_float(gv[i] << 16);
                s += __uint_as_float(xv[rr][i] & 0xffff0000u) * __uint_as_float(gv[i] & 0xffff0000u);
            }
#pragma unroll
            for (int o = 32; o > 0; o >>= 1) s += __shfl_xor(s, o);
            if (lane == r4 + rr) mine = s;
        }
    }
    if (lane < 16) gout[sb * 128 + w * 16 + lane] = 1.0f / (1.0f + __expf(-mine));
}
DI void wo_job(const __hip_bfloat16* __restrict__ woA, const __hip_bfloat16* __restrict__ woB, char* __restrict__ ws, int f, int lane) {
    const int br_ = f / 800, rem = f % 800, nt = rem / 20, ks = rem % 20;
    const unsigned short* src = (const unsigned short*)(br_ ? woB : woA) + (size_t)(ks * 32 + 8 * (lane >> 4)) * DM + nt * 16 + (lane & 15);
    unsigned short we[8];
#pragma unroll
    for (int jj = 0; jj < 8; ++jj) we[jj] = src[jj * DM];
    u32x4 v;
#pragma unroll
    for (int jj = 0; jj < 4; ++jj) v[jj] = (unsigned)we[2 * jj] | ((unsigned)we[2 * jj + 1] << 16);
    gst16(ws + WS_WO + (size_t)f * 1024 + lane * 16, v);
}

__global__ __launch_bounds__(512, 2) void qkv_kernel(const __hip_bfloat16* __restrict__ x, const __hip_bfloat16* __restrict__ wA,
                                                     const __hip_bfloat16* __restrict__ wB, const __hip_bfloat16* __restrict__ woA,
                                                     const __hip_bfloat16* __restrict__ woB, const __hip_bfloat16* __restrict__ gw,
                                                     char* __restrict__ ws) {
    extern __shared__ __attribute__((aligned(16))) char lds[];
    const int tid = threadIdx.x, lane = tid & 63;
    const int w = __builtin_amdgcn_readfirstlane(tid >> 6);
    const int bid = blockIdx.x;

    if (bid >= 240) { gate_job(x, gw, ws, bid - 240, w, lane); return; }
    const int t = (bid & 7) * 30 + (bid >> 3);
    const int tr = t % 120;
    const int n_tile = tr >> 3, m_tile = (t / 120) * 8 + (tr & 7);
    const int m0 = m_tile * 128, n0 = n_tile * 256;
    if (w >= 4) {
        const int lw = w - 4;
        const int row = 8 * lw + (lane >> 3);
        const int ch = (lane & 7) ^ ((row >> 1) & 7);
        const char* asrc = (const char*)(x + (size_t)(m0 + row) * DM + ch * 8);
        const int k = 2 * lw + (lane >> 5);
        const int s16 = lane & 31;
        const int c64 = (s16 >> 2) ^ (k & 3);
        const int col = n0 + c64 * 32 + (s16 & 3) * 8;
        const __hip_bfloat16* wp = (col < N3) ? (wA + col) : (wB + (col - N3));
        const char* bsrc = (const char*)(wp + (size_t)k * N3);
        gemm_stage(asrc, bsrc, (unsigned)(uintptr_t)lds, lw);
        const int f = t * 4 + lw;
        wo_job(woA, woB, ws, f, lane);
        if (f + 960 < 1600) wo_job(woA, woB, ws, f + 960, lane);
    } else {
        const int wm = w & 1, wn = w >> 1;
        const int ncol = n0 + wn * 128;
        const int br = ncol >= N3 ? 1 : 0;
        const int nb = ncol - br * N3;
        const int part = nb / DM;
        const int np = nb - part * DM;
        f32x16 acc[4][2];
#pragma unroll
        for (int i = 0; i < 4; ++i)
#pragma unroll
            for (int j = 0; j < 2; ++j) acc[i][j] = (f32x16){};
        const int r32 = lane & 31, h = lane >> 5;
        if (part == 2) {
            gemm_consume<true>(acc, lds, w, lane);

#pragma unroll
            for (int nt = 0; nt < 4; ++nt) {
                const int nn = np + nt * 32 + r32;
                const int head = nn / DH, d = nn - head * DH;
#pragma unroll
                for (int mt = 0; mt < 2; ++mt) {
                    const int tok0 = m0 + wm * 64 + mt * 32;
                    char* base = ws + WS_KV + (size_t)((br * 32 + head) * 8 + (tok0 >> 8)) * CHUNK + OFF_V + ((tok0 >> 5) & 7) * 1536 + h * 384;
#pragma unroll
                    for (int s = 0; s < 2; ++s) {
                        const f32x16& a = acc[nt][mt];
                        u32x4 v = {pack2(a[8 * s], a[8 * s + 1]), pack2(a[8 * s + 2], a[8 * s + 3]), pack2(a[8 * s + 4], a[8 * s + 5]),
                                   pack2(a[8 * s + 6], a[8 * s + 7])};
                        gst16(base + s * 768 + d * 16, v);
                        if (d < 2) { const unsigned c1 = d ? 0u : 0x3f803f80u; gst16(base + s * 768 + 320 + d * 16, (u32x4){c1, c1, c1, c1}); }
                    }
                }
            }
        } else {
            gemm_consume<false>(acc, lds, w, lane);

            const float sc = part == 0 ? QSCALE : 1.0f;
            const int tok = m0 + wm * 64 + r32;
            char* img = part == 0 ? ws + WS_Q + (size_t)br * 32 * QIMG + tok * 8
                                  : ws + WS_KV + (size_t)(br * 256 + (tok >> 8)) * CHUNK + (tok & 255) * 8;
            const int hstride = part == 0 ? QIMG : 8 * CHUNK, gstride = part == 0 ? 16384 : 2048;
#pragma unroll
            for (int nt = 0; nt < 4; ++nt) {
#pragma unroll
                for (int rp = 0; rp < 4; rp += 2) {
                    char* dst[2];
#pragma unroll
                    for (int e = 0; e < 2; ++e) {
                        const int nn = np + nt * 32 + 8 * (rp + e) + 4 * h;
                        const int head = nn / DH, gq = (nn - head * DH) >> 2;
                        dst[e] = img + (size_t)head * hstride + gq * gstride;
                    }
#pragma unroll
                    for (int mt = 0; mt < 2; ++mt) {
                        const f32x16& a = acc[nt][mt];
                        u32x2 va = {pack2(a[4 * rp] * sc, a[4 * rp + 1] * sc), pack2(a[4 * rp + 2] * sc, a[4 * rp + 3] * sc)};
                        u32x2 vb = {pack2(a[4 * rp + 4] * sc, a[4 * rp + 5] * sc), pack2(a[4 * rp + 6] * sc, a[4 * rp + 7] * sc)};
                        gst_pair(dst[0] + mt * 256, dst[1] + mt * 256, va, vb, lane);
                    }
                }
            }
        }

    }

}

constexpr int A_NSLOT = 4, A_CNT = A_NSLOT * CHUNK, A_LDS = A_CNT + 256;

DI float half_swap_max(float m) {
    auto rr = __builtin_amdgcn_permlane32_swap(__float_as_uint(m), __float_as_uint(m), false, false);
    return __builtin_fmaxf(__uint_as_float(rr[0]), __uint_as_float(rr[1]));
}
DI float rowmax16(const f32x16& s) {
    float a = __builtin_fmaxf(__builtin_fmaxf(s[0], s[1]), s[2]);
#pragma unroll
    for (int r = 3; r < 15; r += 2) a = __builtin_fmaxf(__builtin_fmaxf(a, s[r]), s[r + 1]);
    a = __builtin_fmaxf(a, s[15]);
    return half_swap_max(a);
}
DI void softmax_pv(const f32x16& s, const bf16x8& v0, const bf16x8& v1, f32x16& o) {
    float p[16];
#pragma unroll
    for (int r = 0; r < 16; ++r) p[r] = __builtin_amdgcn_exp2f(s[r]);
    u32x4 p0 = {pack2(p[0], p[1]), pack2(p[2], p[3]), pack2(p[4], p[5]), pack2(p[6], p[7])};
    u32x4 p1 = {pack2(p[8], p[9]), pack2(p[10], p[11]), pack2(p[12], p[13]), pack2(p[14], p[15])};
    o = MFMA32(v0, __builtin_bit_cast(bf16x8, p0), o);
    o = MFMA32(v1, __builtin_bit_cast(bf16x8, p1), o);
}
DI int crow(int r, int hi) { return (r & 3) + 8 * (r >> 2) + 4 * hi; }

DI void attn_slow(f32x16& o, const char* kv, const bf16x8 q1, const bf16x4 q2, int ubeg, int uend, int mode, int qpos, int lane) {
    const int r32 = lane & 31, h = lane >> 5;
    const int vrow = r32 <= 20 ? r32 : 20;
    float m = -INFINITY;
    o = (f32x16){};
    for (int i = uend; i >= ubeg; --i) {
        const char* cb = kv + (size_t)(i >> 3) * CHUNK;
        const int t = i & 7;
        const char* kp = cb + (t * 32 + r32) * 8;
        bf16x8 k1 = cat8(*(const bf16x4*)(kp + h * 4096), *(const bf16x4*)(kp + h * 4096 + 2048));
        bf16x4 k2 = *(const bf16x4*)(kp + 8192);
        bf16x8 v0 = *(const bf16x8*)(cb + OFF_V + t * 1536 + h * 384 + vrow * 16);
        bf16x8 v1 = *(const bf16x8*)(cb + OFF_V + t * 1536 + 768 + h * 384 + vrow * 16);
        f32x16 s = (f32x16){};
        s = MFMA32(k1, q1, s);
        s = MFMA32K8(k2, q2, s);
        if (mode) {
#pragma unroll
            for (int r = 0; r < 16; ++r) {
                const int dlt = qpos - (i * 32 + crow(r, h));
                if (dlt < 0 || dlt >= 256) s[r] = -INFINITY;
            }
        }
        const float mn = __builtin_fmaxf(m, rowmax16(s));
        const float alpha = __builtin_amdgcn_exp2f(m - mn);
        m = mn;
#pragma unroll
        for (int r = 0; r < 16; ++r) { o[r] *= alpha; s[r] -= mn; }
        softmax_pv(s, v0, v1, o);
    }
}

DI void store_o(const f32x16& o, float l, char* oimg, int head, int qrow, int h) {
    const float rl = 1.0f / l;
    char* base = oimg + (size_t)(qrow >> 4) * 20480 + (head * 5 + h) * 128 + (qrow & 15) * 8;
    const u32x2 va = {pack2(o[0] * rl, o[1] * rl), pack2(o[2] * rl, o[3] * rl)}, vb = {pack2(o[4] * rl, o[5] * rl), pack2(o[6] * rl, o[7] * rl)};
    gst_pair(base, base + 256, va, vb, qrow);
    if (h == 0) gst8(base + 512, (u32x2){pack2(o[8] * rl, o[9] * rl), pack2(o[10] * rl, o[11] * rl)});
}
DI float get_l(const f32x16& o) {
    auto rr = __builtin_amdgcn_permlane32_swap(__float_as_uint(o[8]), __float_as_uint(o[8]), false, false);
    return __uint_as_float(rr[1]);
}
DI bool unsafe(const f32x16& o, float l) {
    float z = 0.f, mx = 0.f;
#pragma unroll
    for (int r = 0; r < 12; ++r) z = __builtin_fmaf(o[r], 0.f, z);
#pragma unroll
    for (int r = 0; r < 8; ++r) mx = __builtin_fmaxf(mx, __builtin_fabsf(o[r]));
    return __any(!(l < LMAX) || !(z == 0.f) || !(mx > OMIN));
}
#define EXP4(P, S, i) do { P[i] = __builtin_amdgcn_exp2f(S[i]); P[i + 1] = __builtin_amdgcn_exp2f(S[i + 1]); \
                           P[i + 2] = __builtin_amdgcn_exp2f(S[i + 2]); P[i + 3] = __builtin_amdgcn_exp2f(S[i + 3]); } while (0)
#define STEP(SC, SN, CN, Q1, Q2, KP, VP, VPN) do { \
    float p_[12]; \
    SN = MFMA32(cat8(k1a, k1b), Q1, CN); SBAR(); \
    k1a = lds_b64(KP); k1b = lds_b64((KP) + 2048); \
    EXP4(p_, SC, 0); pb[2] = pack2(pt[0], pt[1]); pb[3] = pack2(pt[2], pt[3]); SBAR(); \
    o = MFMA32(v1, __builtin_bit_cast(bf16x8, pb), o); SBAR(); \
    v1 = lds_b128((VP) + 768); \
    EXP4(p_, SC, 4); pa[0] = pack2(p_[0], p_[1]); pa[1] = pack2(p_[2], p_[3]); SBAR(); \
    SN = MFMA32K8(k2, Q2, SN); SBAR(); \
    k2 = lds_b64((KP) + k2d); \
    EXP4(p_, SC, 8); pa[2] = pack2(p_[4], p_[5]); pa[3] = pack2(p_[6], p_[7]); SBAR(); \
    o = MFMA32(v0, __builtin_bit_cast(bf16x8, pa), o); SBAR(); \
    v0 = lds_b128(VPN); \
    pt[0] = __builtin_amdgcn_exp2f(SC[12]); pt[1] = __builtin_amdgcn_exp2f(SC[13]); \
    pt[2] = __builtin_amdgcn_exp2f(SC[14]); pt[3] = __builtin_amdgcn_exp2f(SC[15]); \
    pb[0] = pack2(p_[8], p_[9]); pb[1] = pack2(p_[10], p_[11]); \
    asm volatile("" : "+v"(pt[0]), "+v"(pt[1]), "+v"(pt[2]), "+v"(pt[3]), "+v"(pb)); SBAR();     \
    } while (0)
#define SETPRIO(x) __builtin_amdgcn_s_setprio(x)
#define DRAIN() do { pb[2] = pack2(pt[0], pt[1]); pb[3] = pack2(pt[2], pt[3]); o = MFMA32(v1, __builtin_bit_cast(bf16x8, pb), o); } while (0)

__global__ __launch_bounds__(512, 2) void attn_kernel(char* __restrict__ ws) {
    extern __shared__ __attribute__((aligned(16))) char lds[];
    const int tid = threadIdx.x, lane = tid & 63, r32 = lane & 31, h = lane >> 5;
    const int w = __builtin_amdgcn_readfirstlane(tid >> 6);
    const int bid = blockIdx.x;
    const int head = (bid & 7) * 4 + (bid >> 6), j = (bid >> 3) & 7;
    const unsigned lds0 = (unsigned)(uintptr_t)lds;
    const char* kvA = ws + WS_KV + (size_t)(head * 8) * CHUNK;
    const char* kvB = ws + WS_KV + (size_t)((32 + head) * 8) * CHUNK;
    const int qrow = j * 256 + w * 32 + r32;

    const int p3 = w < 6 ? 16384 : 0;
    auto issue = [&](int c, int sl) {
        const char* src = c < 8 ? kvA + (size_t)c * CHUNK : kvB + (size_t)(c == 8 ? j : (j > 0 ? j - 1 : 0)) * CHUNK;
        const unsigned dst = lds0 + sl * CHUNK + w * 1024;
        src += w * 1024 + lane * 16;
        glds16(src, (unsigned)__builtin_amdgcn_readfirstlane(dst));
        glds16(src + 8192, (unsigned)__builtin_amdgcn_readfirstlane(dst + 8192));
        glds16(src + p3, (unsigned)__builtin_amdgcn_readfirstlane(dst + p3));
    };
    const char* qp = ws + WS_Q + (size_t)head * QIMG + qrow * 8;
    bf16x4 qa, qb, q2;
    asm volatile("global_load_dwordx2 %0, %3, off\n\tglobal_load_dwordx2 %1, %4, off\n\tglobal_load_dwordx2 %2, %5, off"
                 : "=&v"(qa), "=&v"(qb), "=&v"(q2) : "v"(qp + h * 32768), "v"(qp + h * 32768 + 16384), "v"(qp + 65536) : "memory");
    issue(0, 0);
    issue(1, 1);
    if (tid < 32) *(volatile unsigned*)(lds + A_CNT + tid * 4) = 0u;
    asm volatile("s_waitcnt vmcnt(3) lgkmcnt(0)\n\ts_barrier" : "+v"(qa), "+v"(qb), "+v"(q2) :: "memory");

    issue(2, 2);
    bf16x8 q1 = cat8(qa, qb);
    if (h) q2 = (bf16x4){};
    bf16x8 q1b;
    bf16x4 q2b;
    {
        const char* qpb = qp + (size_t)32 * QIMG;
        q1b = cat8(*(const bf16x4*)(qpb + h * 32768), *(const bf16x4*)(qpb + h * 32768 + 16384));
        q2b = *(const bf16x4*)(qpb + 65536);
    }

    const int vrow = r32 <= 21 ? r32 : 21;
    const int koff = h * 4096 + r32 * 8, k2d = 8192 - h * 4096, voff = OFF_V + h * 384 + vrow * 16;
    constexpr int S1 = (8 % A_NSLOT) * CHUNK, S0 = (9 % A_NSLOT) * CHUNK;
    lds_cptr kb = (lds_cptr)lds + koff, vb = (lds_cptr)lds + voff;
    auto kad = [&](int u) { return kb + ((u < 8 ? S0 : S1) + (u & 7) * 256); };
    auto vad = [&](int u) { return vb + ((u < 8 ? S0 : S1) + (u & 7) * 1536); };

    f32x16 o = (f32x16){};
    f32x16 cc, sa, sb_;
    bf16x8 v0, v1;
    bf16x4 k1a, k1b, k2;
    u32x4 pa, pb = (u32x4){};
    float pt[4] = {0.f, 0.f, 0.f, 0.f};
    {
        lds_cptr kp = kb;
        f32x16 s = (f32x16){};
        s = MFMA32(cat8(lds_b64(kp), lds_b64(kp + 2048)), q1, s);
        s = MFMA32K8(lds_b64(kp + k2d), q2, s);
        const float nm = -(rowmax16(s) + MBIAS);
#pragma unroll
        for (int r = 0; r < 16; ++r) { cc[r] = nm; sa[r] = s[r] + nm; }
        k1a = lds_b64(kp + 256); k1b = lds_b64(kp + 256 + 2048); k2 = lds_b64(kp + 256 + k2d);
        v0 = lds_b128(vb);
        v1 = v0;
    }
    asm volatile("" : "+v"(cc));
#pragma unroll 1
    for (int c = 0; c < 8; ++c) {
        const int slot = c & 3, nslot = (c + 1) & 3;
        lds_cptr kc = kb + slot * CHUNK, vc = vb + slot * CHUNK;
        lds_cptr kn0 = c < 7 ? kb + nslot * CHUNK : kad(w + 8), kn1 = c < 7 ? kb + nslot * CHUNK + 256 : kad(w + 7);
        lds_cptr vn0 = c < 7 ? vb + nslot * CHUNK : vad(w + 8);
        typedef __attribute__((address_space(3))) volatile unsigned* lds_vu;
        const unsigned cL = lds0 + A_CNT + (c + 1) * 4, cD = lds0 + A_CNT + 64 + c * 4;
#define SIGNAL(addr) asm volatile("s_mov_b64 exec, 1\n\tds_add_u32 %0, %1\n\ts_mov_b64 exec, -1" :: "v"(addr), "v"(1u) : "memory")
#define AWAIT(val, addr) do { if (__builtin_expect((unsigned)__builtin_amdgcn_readfirstlane(val) < 8u, 0)) { \
            while ((unsigned)__builtin_amdgcn_readfirstlane(*(lds_vu)(uintptr_t)(addr)) < 8u) __builtin_amdgcn_s_sleep(1); } \
            asm volatile("" ::: "memory"); } while (0)
        SETPRIO(2);
        STEP(sa, sb_, cc, q1, q2, kc + 2 * 256, vc + 0 * 1536, vc + 1 * 1536);
        SIGNAL(cD);
        STEP(sb_, sa, cc, q1, q2, kc + 3 * 256, vc + 1 * 1536, vc + 2 * 1536);
        if (c < 7) asm volatile("s_waitcnt vmcnt(3)" ::: "memory"); else asm volatile("s_waitcnt vmcnt(0)" ::: "memory");
        SIGNAL(cL);
        SETPRIO(1);
        STEP(sa, sb_, cc, q1, q2, kc + 4 * 256, vc + 2 * 1536, vc + 3 * 1536);
        STEP(sb_, sa, cc, q1, q2, kc + 5 * 256, vc + 3 * 1536, vc + 4 * 1536);
        const unsigned vD = *(lds_vu)(uintptr_t)cD;
        SETPRIO(0);
        STEP(sa, sb_, cc, q1, q2, kc + 6 * 256, vc + 4 * 1536, vc + 5 * 1536);
        const unsigned vL = *(lds_vu)(uintptr_t)cL;
        if (c + 3 < 10) {
            AWAIT(vD, cD);
            issue(c + 3, (c + 3) & 3);
        }
        STEP(sb_, sa, cc, q1, q2, kc + 7 * 256, vc + 5 * 1536, vc + 6 * 1536);
        AWAIT(vL, cL);
        SETPRIO(3);
        STEP(sa, sb_, cc, q1, q2, kn0, vc + 6 * 1536, vc + 7 * 1536);
        if (c == 7) {
            q1 = q1b;
            q2 = h ? (bf16x4){} : q2b;
#pragma unroll
            for (int r = 0; r < 16; ++r) cc[r] = crow(r, h) <= r32 ? 0.f : -INFINITY;
            asm volatile("" : "+v"(cc), "+v"(q1), "+v"(q2));
        }
        STEP(sb_, sa, cc, q1, q2, kn1, vc + 7 * 1536, vn0);
    }
    DRAIN();

    {
        float l = get_l(o);
        if (__builtin_expect(unsafe(o, l), 0)) {
            bf16x8 q1s = cat8(*(const bf16x4*)(qp + h * 32768), *(const bf16x4*)(qp + h * 32768 + 16384));
            bf16x4 q2s = *(const bf16x4*)(qp + 65536);
            if (h) q2s = (bf16x4){};
            attn_slow(o, kvA, q1s, q2s, 0, 63, 0, qrow, lane);
            l = get_l(o);
        }
        store_o(o, l, ws + WS_O, head, qrow, h);
    }

    {
        o = (f32x16){};
        const float nm = -(rowmax16(sa) + MBIAS);
#pragma unroll
        for (int r = 0; r < 16; ++r) { sa[r] += nm; cc[r] = nm; }
        {
            pb = (u32x4){};
            pt[0] = pt[1] = pt[2] = pt[3] = 0.f;
            const int kill = j == 0 ? w : -1;
            asm volatile("" : "+v"(cc));
#define KILL(i) do { if (kill == (i)) { _Pragma("unroll") for (int r = 0; r < 16; ++r) cc[r] = -INFINITY; asm volatile("" : "+v"(cc)); } } while (0)
            KILL(0); STEP(sa, sb_, cc, q1, q2, kad(w + 6), vad(w + 8), vad(w + 7));
            KILL(1); STEP(sb_, sa, cc, q1, q2, kad(w + 5), vad(w + 7), vad(w + 6));
                KILL(2); STEP(sa, sb_, cc, q1, q2, kad(w + 4), vad(w + 6), vad(w + 5));
            KILL(3); STEP(sb_, sa, cc, q1, q2, kad(w + 3), vad(w + 5), vad(w + 4));
                KILL(4); STEP(sa, sb_, cc, q1, q2, kad(w + 2), vad(w + 4), vad(w + 3));
            KILL(5); STEP(sb_, sa, cc, q1, q2, kad(w + 1), vad(w + 3), vad(w + 2));
            KILL(6); STEP(sa, sb_, cc, q1, q2, kad(w), vad(w + 2), vad(w + 1));
#pragma unroll
            for (int r = 0; r < 16; ++r) cc[r] = (j > 0 && crow(r, h) > r32) ? nm : -INFINITY;
            STEP(sb_, sa, cc, q1, q2, kad(w), vad(w + 1), vad(w));
            STEP(sa, sb_, cc, q1, q2, kad(w), vad(w), vad(w));
            DRAIN();
        }
        float l = get_l(o);
        if (__builtin_expect(unsafe(o, l), 0)) {
            const int ud = j * 8 + w;
            attn_slow(o, kvB, q1, q2, ud >= 8 ? ud - 8 : 0, ud, 1, qrow, lane);
            l = get_l(o);
        }
        store_o(o, l, ws + WS_O + WS_O_BYTES / 2, head, qrow, h);
    }

}

constexpr int P_STAGE = 36 * 1024, P_LDS = 4 * P_STAGE;

__global__ __launch_bounds__(512, 2) void oproj_kernel(char* __restrict__ ws, __hip_bfloat16* __restrict__ out) {
    extern __shared__ __attribute__((aligned(16))) char lds[];
    const int tid = threadIdx.x, lane = tid & 63;
    const int w = __builtin_amdgcn_readfirstlane(tid >> 6);
    const int bid = blockIdx.x;
    const int mb = (bid & 7) * 4 + (bid >> 6), nb = (bid >> 3) & 7;
    const unsigned lds0 = (unsigned)(uintptr_t)lds;

    const char* src[5];
#pragma unroll
    for (int i = 0; i < 5; ++i) {
        int p = w + 8 * i;
        if (p >= 36) p = w;
        if (p < 16) {
            const int br = p >> 3, mt = (p >> 1) & 3, ks = p & 1;
            src[i] = ws + WS_O + ((size_t)(br * 128 + mb * 4 + mt) * 20 + ks) * 1024 + lane * 16;
        } else {
            const int pb = p - 16, br = pb / 10, nt = (pb % 10) >> 1, ks = pb & 1;
            src[i] = ws + WS_WO + ((size_t)(br * 40 + nb * 5 + nt) * 20 + ks) * 1024 + lane * 16;
        }
    }
    auto piece = [&](int s, int i) {
        if (i < 4 || w < 4) glds16(src[i] + s * 2048, (unsigned)__builtin_amdgcn_readfirstlane(lds0 + (s & 3) * P_STAGE + (w + 8 * i) * 1024));
    };
#pragma unroll
    for (int s = 0; s < 3; ++s)
#pragma unroll
        for (int i = 0; i < 5; ++i) piece(s, i);
    const int br = w >> 2, mh = (w >> 1) & 1, kp = w & 1;
    float g[2];
#pragma unroll
    for (int m = 0; m < 2; ++m) g[m] = ((const float*)(ws + WS_G))[mb * 64 + mh * 32 + m * 16 + (lane & 15)];
    f32x4 acc[2][5];
#pragma unroll
    for (int i = 0; i < 2; ++i)
#pragma unroll
        for (int n = 0; n < 5; ++n) acc[i][n] = (f32x4){};
    const int aoff = ((br * 4 + mh * 2) * 2 + kp) * 1024 + (lane >> 4) * 256 + (lane & 15) * 8;
    const int boff = (16 + br * 10 + kp) * 1024 + lane * 16;

#pragma unroll
    for (int s = 0; s < 10; ++s) {
        if (s <= 7) { if (w < 4) WAIT_BAR(10); else WAIT_BAR(8); }
        else if (s == 8) { if (w < 4) WAIT_BAR(5); else WAIT_BAR(4); }
        else WAIT_BAR(0);
        lds_cptr sb = (lds_cptr)lds + (s & 3) * P_STAGE;
        bf16x8 af[2], bfr[5];
#pragma unroll
        for (int i = 0; i < 2; ++i) af[i] = cat8(lds_b64(sb + aoff + i * 2048), lds_b64(sb + aoff + i * 2048 + 128));
#pragma unroll
        for (int n = 0; n < 5; ++n) bfr[n] = lds_b128(sb + boff + n * 2048);
        if (s + 3 < 10) {
#pragma unroll
            for (int i = 0; i < 5; ++i) piece(s + 3, i);
        }
#pragma unroll
        for (int i = 0; i < 2; ++i)
#pragma unroll
            for (int n = 0; n < 5; ++n) acc[i][n] = MFMA16(bfr[n], af[i], acc[i][n]);
    }

    WAIT_BAR(0);
    f32x4* part = (f32x4*)lds;
#pragma unroll
    for (int m = 0; m < 2; ++m)
#pragma unroll
        for (int n = 0; n < 5; ++n) part[(w * 10 + m * 5 + n) * 64 + lane] = acc[m][n];
    __syncthreads();
    const int qd = br * 2 + kp;
#pragma unroll
    for (int i = 0; i < 3; ++i) {
        const int tl = qd + 4 * i;
        if (tl < 10) {
            const int m = tl >= 5 ? 1 : 0, n = tl - 5 * m;
            const int row = mb * 64 + mh * 32 + m * 16 + (lane & 15);
            const float gg = m ? g[1] : g[0];
            const f32x4 a = part[((mh * 2 + 0) * 10 + tl) * 64 + lane] + part[((mh * 2 + 1) * 10 + tl) * 64 + lane];
            const f32x4 b = part[((4 + mh * 2 + 0) * 10 + tl) * 64 + lane] + part[((4 + mh * 2 + 1) * 10 + tl) * 64 + lane];
            const f32x4 v = gg * a + (1.0f - gg) * b;
            u32x2 pk = {pack2(v[0], v[1]), pack2(v[2], v[3])};
            *(u32x2*)(out + (size_t)row * DM + nb * 80 + n * 16 + (lane >> 4) * 4) = pk;
        }
    }

}

extern "C" void kernel_launch(void* const* d_in, const int* in_sizes, int n_in, void* d_out, int out_size, void* d_ws, size_t ws_size,
                              hipStream_t stream) {
    const __hip_bfloat16* x = (const __hip_bfloat16*)d_in[0];
    const __hip_bfloat16* wA = (const __hip_bfloat16*)d_in[1];
    const __hip_bfloat16* woA = (const __hip_bfloat16*)d_in[2];
    const __hip_bfloat16* wB = (const __hip_bfloat16*)d_in[4];
    const __hip_bfloat16* woB = (const __hip_bfloat16*)d_in[5];
    const __hip_bfloat16* gw = (const __hip_bfloat16*)d_in[6];
    char* ws = (char*)d_ws;
    (void)hipFuncSetAttribute((const void*)qkv_kernel, hipFuncAttributeMaxDynamicSharedMemorySize, G_LDS);
    (void)hipFuncSetAttribute((const void*)attn_kernel, hipFuncAttributeMaxDynamicSharedMemorySize, A_LDS);
    (void)hipFuncSetAttribute((const void*)oproj_kernel, hipFuncAttributeMaxDynamicSharedMemorySize, P_LDS);
    qkv_kernel<<<256, 512, G_LDS, stream>>>(x, wA, wB, woA, woB, gw, ws);
    attn_kernel<<<256, 512, A_LDS, stream>>>(ws);
    oproj_kernel<<<256, 512, P_LDS, stream>>>(ws, (__hip_bfloat16*)d_out);
}
```

```cpp
#include <hip/hip_runtime.h>
#include <hip/hip_bf16.h>
#include <stdint.h>

typedef __attribute__((ext_vector_type(8))) short bf16x8;
typedef __attribute__((ext_vector_type(4))) short bf16x4;
typedef __attribute__((ext_vector_type(2))) __bf16 bf16v2;
typedef __attribute__((ext_vector_type(2))) float f32x2;
typedef __attribute__((ext_vector_type(16))) float f32x16;
typedef __attribute__((ext_vector_type(4))) float f32x4;
typedef __attribute__((ext_vector_type(4))) unsigned u32x4;
typedef __attribute__((ext_vector_type(2))) unsigned u32x2;
typedef __attribute__((address_space(3))) const char* lds_cptr;
typedef short v4i16_t __attribute__((ext_vector_type(4)));
typedef __attribute__((address_space(1))) unsigned gu32;
typedef __attribute__((address_space(1))) unsigned long long gu64;

#define DI __device__ __forceinline__
#define MFMA32(a, b, c) __builtin_amdgcn_mfma_f32_32x32x16_bf16((a), (b), (c), 0, 0, 0)
#define MFMA32K8(a, b, c) __builtin_amdgcn_mfma_f32_32x32x8bf16_1k((a), (b), (c), 0, 0, 0)
#define MFMA16(a, b, c) __builtin_amdgcn_mfma_f32_16x16x32_bf16((a), (b), (c), 0, 0, 0)
#define SBAR() __builtin_amdgcn_sched_barrier(0)

constexpr int SEQ = 2048, DM = 640, NH = 32, DH = 20, N3 = 1920;
constexpr int CHUNK = 22 * 1024;
constexpr int OFF_V = 10240;
constexpr size_t WS_KV = 0;
constexpr size_t WS_KV_BYTES = (size_t)2 * 32 * 8 * CHUNK;
constexpr int QIMG = 81920;
constexpr size_t WS_Q = WS_KV + WS_KV_BYTES;
constexpr size_t WS_Q_BYTES = (size_t)2 * 32 * QIMG;
constexpr size_t WS_O = WS_Q + WS_Q_BYTES;
constexpr size_t WS_O_BYTES = (size_t)2 * 128 * 20 * 1024;
constexpr size_t WS_WO = WS_O + WS_O_BYTES;
constexpr size_t WS_WO_BYTES = (size_t)2 * 40 * 20 * 1024;
constexpr size_t WS_G = WS_WO + WS_WO_BYTES;

constexpr float QSCALE = 0.22360679774997896f * 1.4426950408889634f;
constexpr float MBIAS = 96.0f;
constexpr float LMAX = 1.3292280e36f;
constexpr float OMIN = 7.5231638e-37f;

DI unsigned pack2(float lo, float hi) {
    f32x2 v = {lo, hi};
    bf16v2 b = __builtin_convertvector(v, bf16v2);
    return __builtin_bit_cast(unsigned, b);
}

DI void glds16(const void* g, unsigned lds_base) {
    unsigned sv;
    asm volatile("s_mov_b32 %0, m0\n\ts_mov_b32 m0, %2\n\ts_nop 0\n\tglobal_load_lds_dwordx4 %1, off\n\ts_mov_b32 m0, %0"
                 : "=&s"(sv) : "v"(g), "s"(lds_base) : "memory");
}
#define WAIT_BAR(N) asm volatile("s_waitcnt vmcnt(" #N ") lgkmcnt(0)\n\ts_barrier" ::: "memory")
#define LGKM_BAR() asm volatile("s_waitcnt lgkmcnt(0)\n\ts_barrier" ::: "memory")

DI bf16x8 lds_b128(lds_cptr p) { return *(const __attribute__((address_space(3))) bf16x8*)p; }
DI bf16x4 lds_b64(lds_cptr p) { return *(const __attribute__((address_space(3))) bf16x4*)p; }
DI bf16x4 lds_tr(lds_cptr p) {
    return __builtin_bit_cast(bf16x4, __builtin_amdgcn_ds_read_tr16_b64_v4i16((__attribute__((address_space(3))) v4i16_t*)p));
}
DI void gst8(char* p, u32x2 v) {
    __hip_atomic_store((gu64*)p, (unsigned long long)v[0] | ((unsigned long long)v[1] << 32), __ATOMIC_RELAXED, __HIP_MEMORY_SCOPE_AGENT);
}
DI void gst16(char* p, u32x4 v) { asm volatile("global_store_dwordx4 %0, %1, off sc1\n\ts_nop 1" :: "v"(p), "v"(v) : "memory"); }
DI void gst_pair(char* pa, char* pb, u32x2 a, u32x2 b, int lane) {
    const bool odd = lane & 1;
    const unsigned g0 = odd ? a[0] : b[0], g1 = odd ? a[1] : b[1];
    const unsigned y0 = __builtin_amdgcn_mov_dpp(g0, 0xB1, 0xF, 0xF, true), y1 = __builtin_amdgcn_mov_dpp(g1, 0xB1, 0xF, 0xF, true);
    const u32x4 v = odd ? (u32x4){y0, y1, b[0], b[1]} : (u32x4){a[0], a[1], y0, y1};
    gst16(odd ? pb - 8 : pa, v);
}
DI bf16x8 cat8(bf16x4 lo, bf16x4 hi) { return __builtin_shufflevector(lo, hi, 0, 1, 2, 3, 4, 5, 6, 7); }

constexpr int G_STAGE = 48 * 1024, G_BOFF = 16 * 1024, G_LDS = 3 * G_STAGE;

template <bool ISV>
DI void gemm_consume(f32x16 (&acc)[4][2], char* lds, int w, int lane) {
    const int r32 = lane & 31, h = lane >> 5;
    const int wm = w & 1, wn = w >> 1;
    const int arow = wm * 64 + r32;
    const int asw = (arow >> 1) & 7;
    int aoff[4];
#pragma unroll
    for (int kk = 0; kk < 4; ++kk) aoff[kk] = arow * 128 + (((2 * kk + h) ^ asw) << 4);
    const int g = lane >> 4, q = (lane & 15) >> 2, p = lane & 3;
    int boff[4];
#pragma unroll
    for (int nt = 0; nt < 4; ++nt) boff[nt] = G_BOFF + (8 * (g >> 1) + q) * 512 + (((4 * wn + nt) ^ q) << 6) + (16 * (g & 1) + 4 * p) * 2;
    bf16x8 a0[2], b0[4], a1[2], b1[4];
#define MM(A, B, nt, mt) do { if (ISV) acc[nt][mt] = MFMA32(A[mt], B[nt], acc[nt][mt]); else acc[nt][mt] = MFMA32(B[nt], A[mt], acc[nt][mt]); } while (0)
#define RB(BN, sb, kk, nt) BN[nt] = cat8(lds_tr(sb + boff[nt] + (kk) * 8192), lds_tr(sb + boff[nt] + (kk) * 8192 + 2048))
#define RA(AN, sb, kk) AN[0] = lds_b128(sb + aoff[kk]), AN[1] = lds_b128(sb + aoff[kk] + 4096)
#define GMMA(A, B, AN, BN, sb, kk) do { \
        MM(A, B, 0, 0); SBAR(); RA(AN, sb, kk); SBAR(); \
        MM(A, B, 0, 1); SBAR(); RB(BN, sb, kk, 0); SBAR(); \
        MM(A, B, 1, 0); SBAR(); RB(BN, sb, kk, 1); SBAR(); \
        MM(A, B, 1, 1); SBAR(); RB(BN, sb, kk, 2); SBAR(); \
        MM(A, B, 2, 0); SBAR(); RB(BN, sb, kk, 3); SBAR(); \
        MM(A, B, 2, 1); MM(A, B, 3, 0); MM(A, B, 3, 1); SBAR(); } while (0)
    LGKM_BAR();

    {
        lds_cptr sb = (lds_cptr)lds;
        a0[0] = lds_b128(sb + aoff[0]); a0[1] = lds_b128(sb + aoff[0] + 4096);
#pragma unroll
        for (int nt = 0; nt < 4; ++nt) b0[nt] = cat8(lds_tr(sb + boff[nt]), lds_tr(sb + boff[nt] + 2048));
    }
    int sl = 0;
#pragma unroll 1
    for (int it = 0; it < 10; ++it) {
        const int nsl = sl == 2 ? 0 : sl + 1;
        lds_cptr sc = (lds_cptr)lds + sl * G_STAGE, sn = (lds_cptr)lds + nsl * G_STAGE;
        GMMA(a0, b0, a1, b1, sc, 1);
        GMMA(a1, b1, a0, b0, sc, 2);
        GMMA(a0, b0, a1, b1, sc, 3);
        if (it < 9) LGKM_BAR();
        GMMA(a1, b1, a0, b0, sn, 0);
        sl = nsl;
    }
#undef MM
#undef RB
#undef RA
#undef GMMA
}

DI void gemm_stage(const char* asrc, const char* bsrc, unsigned lds0, int lw) {
    auto issue = [&](int s, int sl) {
        const unsigned base = lds0 + sl * G_STAGE + lw * 1024;
#pragma unroll
        for (int i = 0; i < 4; ++i) glds16(asrc + s * 128 + i * (32 * DM * 2), (unsigned)__builtin_amdgcn_readfirstlane(base + i * 4096));
#pragma unroll
        for (int i = 0; i < 8; ++i) glds16(bsrc + (size_t)(s * 64 + i * 8) * N3 * 2, (unsigned)__builtin_amdgcn_readfirstlane(base + G_BOFF + i * 4096));
    };
    issue(0, 0);
    issue(1, 1);
    WAIT_BAR(12);
    issue(2, 2);
    int sl = 0;
#pragma unroll 1
    for (int it = 0; it < 9; ++it) {
        if (it <= 7) WAIT_BAR(12); else WAIT_BAR(0);
        if (it + 3 < 10) issue(it + 3, sl);
        sl = sl == 2 ? 0 : sl + 1;
    }
}

DI void gate_job(const __hip_bfloat16* __restrict__ x, const __hip_bfloat16* __restrict__ gw, char* __restrict__ ws, int sb, int w, int lane) {
    const unsigned short* xs = (const unsigned short*)x;
    const unsigned short* gs = (const unsigned short*)gw;
    float* gout = (float*)(ws + WS_G);
    unsigned gv[5];
#pragma unroll
    for (int i = 0; i < 5; ++i) gv[i] = *(const unsigned*)(gs + lane * 10 + 2 * i);
    float mine = 0.f;
#pragma unroll
    for (int r4 = 0; r4 < 16; r4 += 8) {
        unsigned xv[8][5];
#pragma unroll
        for (int rr = 0; rr < 8; ++rr)
#pragma unroll
            for (int i = 0; i < 5; ++i) xv[rr][i] = *(const unsigned*)(xs + (size_t)(sb * 128 + w * 16 + r4 + rr) * DM + lane * 10 + 2 * i);
#pragma unroll
        for (int rr = 0; rr < 8; ++rr) {
            float s = 0.f;
#pragma unroll
            for (int i = 0; i < 5; ++i) {
                s += __uint_as_float(xv[rr][i] << 16) * __uint_as_float(gv[i] << 16);
                s += __uint_as_float(xv[rr][i] & 0xffff0000u) * __uint_as_float(gv[i] & 0xffff0000u);
            }
#pragma unroll
            for (int o = 32; o > 0; o >>= 1) s += __shfl_xor(s, o);
            if (lane == r4 + rr) mine = s;
        }
    }
    if (lane < 16) gout[sb * 128 + w * 16 + lane] = 1.0f / (1.0f + __expf(-mine));
}
DI void wo_job(const __hip_bfloat16* __restrict__ woA, const __hip_bfloat16* __restrict__ woB, char* __restrict__ ws, int f, int lane) {
    const int br_ = f / 800, rem = f % 800, nt = rem / 20, ks = rem % 20;
    const unsigned short* src = (const unsigned short*)(br_ ? woB : woA) + (size_t)(ks * 32 + 8 * (lane >> 4)) * DM + nt * 16 + (lane & 15);
    unsigned short we[8];
#pragma unroll
    for (int jj = 0; jj < 8; ++jj) we[jj] = src[jj * DM];
    u32x4 v;
#pragma unroll
    for (int jj = 0; jj < 4; ++jj) v[jj] = (unsigned)we[2 * jj] | ((unsigned)we[2 * jj + 1] << 16);
    gst16(ws + WS_WO + (size_t)f * 1024 + lane * 16, v);
}

__global__ __launch_bounds__(512, 2) void qkv_kernel(const __hip_bfloat16* __restrict__ x, const __hip_bfloat16* __restrict__ wA,
                                                     const __hip_bfloat16* __restrict__ wB, const __hip_bfloat16* __restrict__ woA,
                                                     const __hip_bfloat16* __restrict__ woB, const __hip_bfloat16* __restrict__ gw,
                                                     char* __restrict__ ws) {
    extern __shared__ __attribute__((aligned(16))) char lds[];
    const int tid = threadIdx.x, lane = tid & 63;
    const int w = __builtin_amdgcn_readfirstlane(tid >> 6);
    const int bid = blockIdx.x;

    if (bid >= 240) { gate_job(x, gw, ws, bid - 240, w, lane); return; }
    const int t = (bid & 7) * 30 + (bid >> 3);
    const int tr = t % 120;
    const int n_tile = tr >> 3, m_tile = (t / 120) * 8 + (tr & 7);
    const int m0 = m_tile * 128, n0 = n_tile * 256;
    if (w >= 4) {
        const int lw = w - 4;
        const int row = 8 * lw + (lane >> 3);
        const int ch = (lane & 7) ^ ((row >> 1) & 7);
        const char* asrc = (const char*)(x + (size_t)(m0 + row) * DM + ch * 8);
        const int k = 2 * lw + (lane >> 5);
        const int s16 = lane & 31;
        const int c64 = (s16 >> 2) ^ (k & 3);
        const int col = n0 + c64 * 32 + (s16 & 3) * 8;
        const __hip_bfloat16* wp = (col < N3) ? (wA + col) : (wB + (col - N3));
        const char* bsrc = (const char*)(wp + (size_t)k * N3);
        gemm_stage(asrc, bsrc, (unsigned)(uintptr_t)lds, lw);
        const int f = t * 4 + lw;
        wo_job(woA, woB, ws, f, lane);
        if (f + 960 < 1600) wo_job(woA, woB, ws, f + 960, lane);
    } else {
        const int wm = w & 1, wn = w >> 1;
        const int ncol = n0 + wn * 128;
        const int br = ncol >= N3 ? 1 : 0;
        const int nb = ncol - br * N3;
        const int part = nb / DM;
        const int np = nb - part * DM;
        f32x16 acc[4][2];
#pragma unroll
        for (int i = 0; i < 4; ++i)
#pragma unroll
            for (int j = 0; j < 2; ++j) acc[i][j] = (f32x16){};
        const int r32 = lane & 31, h = lane >> 5;
        if (part == 2) {
            gemm_consume<true>(acc, lds, w, lane);

#pragma unroll
            for (int nt = 0; nt < 4; ++nt) {
                const int nn = np + nt * 32 + r32;
                const int head = nn / DH, d = nn - head * DH;
#pragma unroll
                for (int mt = 0; mt < 2; ++mt) {
                    const int tok0 = m0 + wm * 64 + mt * 32;
                    char* base = ws + WS_KV + (size_t)((br * 32 + head) * 8 + (tok0 >> 8)) * CHUNK + OFF_V + ((tok0 >> 5) & 7) * 1536 + h * 384;
#pragma unroll
                    for (int s = 0; s < 2; ++s) {
                        const f32x16& a = acc[nt][mt];
                        u32x4 v = {pack2(a[8 * s], a[8 * s + 1]), pack2(a[8 * s + 2], a[8 * s + 3]), pack2(a[8 * s + 4], a[8 * s + 5]),
                                   pack2(a[8 * s + 6], a[8 * s + 7])};
                        gst16(base + s * 768 + d * 16, v);
                        if (d < 2) { const unsigned c1 = d ? 0u : 0x3f803f80u; gst16(base + s * 768 + 320 + d * 16, (u32x4){c1, c1, c1, c1}); }
                    }
                }
            }
        } else {
            gemm_consume<false>(acc, lds, w, lane);

            const float sc = part == 0 ? QSCALE : 1.0f;
            const int tok = m0 + wm * 64 + r32;
            char* img = part == 0 ? ws + WS_Q + (size_t)br * 32 * QIMG + tok * 8
                                  : ws + WS_KV + (size_t)(br * 256 + (tok >> 8)) * CHUNK + (tok & 255) * 8;
            const int hstride = part == 0 ? QIMG : 8 * CHUNK, gstride = part == 0 ? 16384 : 2048;
#pragma unroll
            for (int nt = 0; nt < 4; ++nt) {
#pragma unroll
                for (int rp = 0; rp < 4; rp += 2) {
                    char* dst[2];
#pragma unroll
                    for (int e = 0; e < 2; ++e) {
                        const int nn = np + nt * 32 + 8 * (rp + e) + 4 * h;
                        const int head = nn / DH, gq = (nn - head * DH) >> 2;
                        dst[e] = img + (size_t)head * hstride + gq * gstride;
                    }
#pragma unroll
                    for (int mt = 0; mt < 2; ++mt) {
                        const f32x16& a = acc[nt][mt];
                        u32x2 va = {pack2(a[4 * rp] * sc, a[4 * rp + 1] * sc), pack2(a[4 * rp + 2] * sc, a[4 * rp + 3] * sc)};
                        u32x2 vb = {pack2(a[4 * rp + 4] * sc, a[4 * rp + 5] * sc), pack2(a[4 * rp + 6] * sc, a[4 * rp + 7] * sc)};
                        gst_pair(dst[0] + mt * 256, dst[1] + mt * 256, va, vb, lane);
                    }
                }
            }
        }

    }

}

constexpr int A_NSLOT = 4, A_CNT = A_NSLOT * CHUNK, A_LDS = A_CNT + 256;

DI float half_swap_max(float m) {
    auto rr = __builtin_amdgcn_permlane32_swap(__float_as_uint(m), __float_as_uint(m), false, false);
    return __builtin_fmaxf(__uint_as_float(rr[0]), __uint_as_float(rr[1]));
}
DI float rowmax16(const f32x16& s) {
    float a = __builtin_fmaxf(__builtin_fmaxf(s[0], s[1]), s[2]);
#pragma unroll
    for (int r = 3; r < 15; r += 2) a = __builtin_fmaxf(__builtin_fmaxf(a, s[r]), s[r + 1]);
    a = __builtin_fmaxf(a, s[15]);
    return half_swap_max(a);
}
DI void softmax_pv(const f32x16& s, const bf16x8& v0, const bf16x8& v1, f32x16& o) {
    float p[16];
#pragma unroll
    for (int r = 0; r < 16; ++r) p[r] = __builtin_amdgcn_exp2f(s[r]);
    u32x4 p0 = {pack2(p[0], p[1]), pack2(p[2], p[3]), pack2(p[4], p[5]), pack2(p[6], p[7])};
    u32x4 p1 = {pack2(p[8], p[9]), pack2(p[10], p[11]), pack2(p[12], p[13]), pack2(p[14], p[15])};
    o = MFMA32(v0, __builtin_bit_cast(bf16x8, p0), o);
    o = MFMA32(v1, __builtin_bit_cast(bf16x8, p1), o);
}
DI int crow(int r, int hi) { return (r & 3) + 8 * (r >> 2) + 4 * hi; }

DI void attn_slow(f32x16& o, const char* kv, const bf16x8 q1, const bf16x4 q2, int ubeg, int uend, int mode, int qpos, int lane) {
    const int r32 = lane & 31, h = lane >> 5;
    const int vrow = r32 <= 20 ? r32 : 20;
    float m = -INFINITY;
    o = (f32x16){};
    for (int i = uend; i >= ubeg; --i) {
        const char* cb = kv + (size_t)(i >> 3) * CHUNK;
        const int t = i & 7;
        const char* kp = cb + (t * 32 + r32) * 8;
        bf16x8 k1 = cat8(*(const bf16x4*)(kp + h * 4096), *(const bf16x4*)(kp + h * 4096 + 2048));
        bf16x4 k2 = *(const bf16x4*)(kp + 8192);
        bf16x8 v0 = *(const bf16x8*)(cb + OFF_V + t * 1536 + h * 384 + vrow * 16);
        bf16x8 v1 = *(const bf16x8*)(cb + OFF_V + t * 1536 + 768 + h * 384 + vrow * 16);
        f32x16 s = (f32x16){};
        s = MFMA32(k1, q1, s);
        s = MFMA32K8(k2, q2, s);
        if (mode) {
#pragma unroll
            for (int r = 0; r < 16; ++r) {
                const int dlt = qpos - (i * 32 + crow(r, h));
                if (dlt < 0 || dlt >= 256) s[r] = -INFINITY;
            }
        }
        const float mn = __builtin_fmaxf(m, rowmax16(s));
        const float alpha = __builtin_amdgcn_exp2f(m - mn);
        m = mn;
#pragma unroll
        for (int r = 0; r < 16; ++r) { o[r] *= alpha; s[r] -= mn; }
        softmax_pv(s, v0, v1, o);
    }
}

DI void store_o(const f32x16& o, float l, char* oimg, int head, int qrow, int h) {
    const float rl = 1.0f / l;
    char* base = oimg + (size_t)(qrow >> 4) * 20480 + (head * 5 + h) * 128 + (qrow & 15) * 8;
    const u32x2 va = {pack2(o[0] * rl, o[1] * rl), pack2(o[2] * rl, o[3] * rl)}, vb = {pack2(o[4] * rl, o[5] * rl), pack2(o[6] * rl, o[7] * rl)};
    gst_pair(base, base + 256, va, vb, qrow);
    if (h == 0) gst8(base + 512, (u32x2){pack2(o[8] * rl, o[9] * rl), pack2(o[10] * rl, o[11] * rl)});
}
DI float get_l(const f32x16& o) {
    auto rr = __builtin_amdgcn_permlane32_swap(__float_as_uint(o[8]), __float_as_uint(o[8]), false, false);
    return __uint_as_float(rr[1]);
}
DI bool unsafe(const f32x16& o, float l) {
    float z = 0.f, mx = 0.f;
#pragma unroll
    for (int r = 0; r < 12; ++r) z = __builtin_fmaf(o[r], 0.f, z);
#pragma unroll
    for (int r = 0; r < 8; ++r) mx = __builtin_fmaxf(mx, __builtin_fabsf(o[r]));
    return __any(!(l < LMAX) || !(z == 0.f) || !(mx > OMIN));
}
#define EXP4(P, S, i) do { P[i] = __builtin_amdgcn_exp2f(S[i]); P[i + 1] = __builtin_amdgcn_exp2f(S[i + 1]); \
                           P[i + 2] = __builtin_amdgcn_exp2f(S[i + 2]); P[i + 3] = __builtin_amdgcn_exp2f(S[i + 3]); } while (0)
#define STEP(SC, SN, CN, Q1, Q2, KP, VP, VPN) do { \
    float p_[12]; \
    SN = MFMA32(cat8(k1a, k1b), Q1, CN); SBAR(); \
    k1a = lds_b64(KP); k1b = lds_b64((KP) + 2048); \
    EXP4(p_, SC, 0); pb[2] = pack2(pt[0], pt[1]); pb[3] = pack2(pt[2], pt[3]); SBAR(); \
    o = MFMA32(v1, __builtin_bit_cast(bf16x8, pb), o); SBAR(); \
    v1 = lds_b128((VP) + 768); \
    EXP4(p_, SC, 4); pa[0] = pack2(p_[0], p_[1]); pa[1] = pack2(p_[2], p_[3]); SBAR(); \
    SN = MFMA32K8(k2, Q2, SN); SBAR(); \
    k2 = lds_b64((KP) + k2d); \
    EXP4(p_, SC, 8); pa[2] = pack2(p_[4], p_[5]); pa[3] = pack2(p_[6], p_[7]); SBAR(); \
    o = MFMA32(v0, __builtin_bit_cast(bf16x8, pa), o); SBAR(); \
    v0 = lds_b128(VPN); \
    pt[0] = __builtin_amdgcn_exp2f(SC[12]); pt[1] = __builtin_amdgcn_exp2f(SC[13]); \
    pt[2] = __builtin_amdgcn_exp2f(SC[14]); pt[3] = __builtin_amdgcn_exp2f(SC[15]); \
    pb[0] = pack2(p_[8], p_[9]); pb[1] = pack2(p_[10], p_[11]); \
    asm volatile("" : "+v"(pt[0]), "+v"(pt[1]), "+v"(pt[2]), "+v"(pt[3]), "+v"(pb)); SBAR();     \
    } while (0)
#define SETPRIO(x) __builtin_amdgcn_s_setprio(x)
#define DRAIN() do { pb[2] = pack2(pt[0], pt[1]); pb[3] = pack2(pt[2], pt[3]); o = MFMA32(v1, __builtin_bit_cast(bf16x8, pb), o); } while (0)

__global__ __launch_bounds__(512, 2) void attn_kernel(char* __restrict__ ws) {
    extern __shared__ __attribute__((aligned(16))) char lds[];
    const int tid = threadIdx.x, lane = tid & 63, r32 = lane & 31, h = lane >> 5;
    const int w = __builtin_amdgcn_readfirstlane(tid >> 6);
    const int bid = blockIdx.x;
    const int head = (bid & 7) * 4 + (bid >> 6), j = (bid >> 3) & 7;
    const unsigned lds0 = (unsigned)(uintptr_t)lds;
    const char* kvA = ws + WS_KV + (size_t)(head * 8) * CHUNK;
    const char* kvB = ws + WS_KV + (size_t)((32 + head) * 8) * CHUNK;
    const int qrow = j * 256 + w * 32 + r32;

    const int p3 = w < 6 ? 16384 : 0;
    auto issue = [&](int c, int sl) {
        const char* src = c < 8 ? kvA + (size_t)c * CHUNK : kvB + (size_t)(c == 8 ? j : (j > 0 ? j - 1 : 0)) * CHUNK;
        const unsigned dst = lds0 + sl * CHUNK + w * 1024;
        src += w * 1024 + lane * 16;
        glds16(src, (unsigned)__builtin_amdgcn_readfirstlane(dst));
        glds16(src + 8192, (unsigned)__builtin_amdgcn_readfirstlane(dst + 8192));
        glds16(src + p3, (unsigned)__builtin_amdgcn_readfirstlane(dst + p3));
    };
    const char* qp = ws + WS_Q + (size_t)head * QIMG + qrow * 8;
    bf16x4 qa, qb, q2;
    asm volatile("global_load_dwordx2 %0, %3, off\n\tglobal_load_dwordx2 %1, %4, off\n\tglobal_load_dwordx2 %2, %5, off"
                 : "=&v"(qa), "=&v"(qb), "=&v"(q2) : "v"(qp + h * 32768), "v"(qp + h * 32768 + 16384), "v"(qp + 65536) : "memory");
    issue(0, 0);
    issue(1, 1);
    if (tid < 32) *(__attribute__((address_space(3))) volatile unsigned*)(uintptr_t)(lds0 + A_CNT + tid * 4) = 0u;
    asm volatile("s_waitcnt vmcnt(3) lgkmcnt(0)\n\ts_barrier" : "+v"(qa), "+v"(qb), "+v"(q2) :: "memory");

    issue(2, 2);
    bf16x8 q1 = cat8(qa, qb);
    if (h) q2 = (bf16x4){};
    bf16x8 q1b = q1;
    bf16x4 q2b = q2;

    const int vrow = r32 <= 21 ? r32 : 21;
    const int koff = h * 4096 + r32 * 8, k2d = 8192 - h * 4096, voff = OFF_V + h * 384 + vrow * 16;
    constexpr int S1 = (8 % A_NSLOT) * CHUNK, S0 = (9 % A_NSLOT) * CHUNK;
    lds_cptr kb = (lds_cptr)lds + koff, vb = (lds_cptr)lds + voff;
    auto kad = [&](int u) { return kb + ((u < 8 ? S0 : S1) + (u & 7) * 256); };
    auto vad = [&](int u) { return vb + ((u < 8 ? S0 : S1) + (u & 7) * 1536); };

    f32x16 o = (f32x16){};
    f32x16 cc, sa, sb_;
    bf16x8 v0, v1;
    bf16x4 k1a, k1b, k2;
    u32x4 pa, pb = (u32x4){};
    float pt[4] = {0.f, 0.f, 0.f, 0.f};
    {
        lds_cptr kp = kb;
        f32x16 s = (f32x16){};
        s = MFMA32(cat8(lds_b64(kp), lds_b64(kp + 2048)), q1, s);
        s = MFMA32K8(lds_b64(kp + k2d), q2, s);
        k1a = lds_b64(kp + 256); k1b = lds_b64(kp + 256 + 2048); k2 = lds_b64(kp + 256 + k2d);
        f32x16 s1 = MFMA32(cat8(k1a, k1b), q1, o);
        s1 = MFMA32K8(k2, q2, s1);
#pragma unroll
        for (int r = 0; r < 16; ++r) s1[r] = __builtin_fmaxf(s1[r], s[r]);
        const float nm = -(rowmax16(s1) + MBIAS);
#pragma unroll
        for (int r = 0; r < 16; ++r) { cc[r] = nm; sa[r] = s[r] + nm; }
        v0 = lds_b128(vb);
        v1 = v0;
    }
    asm volatile("" : "+v"(cc));
#pragma unroll 1
    for (int c = 0; c < 8; ++c) {
        const int slot = c & 3, nslot = (c + 1) & 3;
        lds_cptr kc = kb + slot * CHUNK, vc = vb + slot * CHUNK;
        lds_cptr kn0 = c < 7 ? kb + nslot * CHUNK : kad(w + 8), kn1 = c < 7 ? kb + nslot * CHUNK + 256 : kad(w + 7);
        lds_cptr vn0 = c < 7 ? vb + nslot * CHUNK : vad(w + 8);
        typedef __attribute__((address_space(3))) volatile unsigned* lds_vu;
        const unsigned cL = lds0 + A_CNT + (c + 1) * 4, cD = lds0 + A_CNT + 64 + c * 4;
#define SIGNAL(addr) asm volatile("s_mov_b64 exec, 1\n\tds_add_u32 %0, %1\n\ts_mov_b64 exec, -1" :: "v"(addr), "v"(1u) : "memory")
#define AWAIT(val, addr) do { if (__builtin_expect((unsigned)__builtin_amdgcn_readfirstlane(val) < 8u, 0)) { \
            while ((unsigned)__builtin_amdgcn_readfirstlane(*(lds_vu)(uintptr_t)(addr)) < 8u) __builtin_amdgcn_s_sleep(1); } \
            asm volatile("" ::: "memory"); } while (0)
        SETPRIO(2);
        STEP(sa, sb_, cc, q1, q2, kc + 2 * 256, vc + 0 * 1536, vc + 1 * 1536);
        SIGNAL(cD);
        STEP(sb_, sa, cc, q1, q2, kc + 3 * 256, vc + 1 * 1536, vc + 2 * 1536);
        if (c < 7) asm volatile("s_waitcnt vmcnt(3)" ::: "memory"); else asm volatile("s_waitcnt vmcnt(0)" ::: "memory");
        SIGNAL(cL);
        SETPRIO(1);
        STEP(sa, sb_, cc, q1, q2, kc + 4 * 256, vc + 2 * 1536, vc + 3 * 1536);
        STEP(sb_, sa, cc, q1, q2, kc + 5 * 256, vc + 3 * 1536, vc + 4 * 1536);
        const unsigned vD = *(lds_vu)(uintptr_t)cD;
        SETPRIO(0);
        STEP(sa, sb_, cc, q1, q2, kc + 6 * 256, vc + 4 * 1536, vc + 5 * 1536);
        const unsigned vL = *(lds_vu)(uintptr_t)cL;
        if (c + 3 < 10) {
            AWAIT(vD, cD);
            if (c == 4) {
                const char* qpb = qp + (size_t)32 * QIMG;
                q1b = cat8(*(const bf16x4*)(qpb + h * 32768), *(const bf16x4*)(qpb + h * 32768 + 16384));
                q2b = *(const bf16x4*)(qpb + 65536);
            }
            issue(c + 3, (c + 3) & 3);
        }
        STEP(sb_, sa, cc, q1, q2, kc + 7 * 256, vc + 5 * 1536, vc + 6 * 1536);
        AWAIT(vL, cL);
        SETPRIO(3);
        STEP(sa, sb_, cc, q1, q2, kn0, vc + 6 * 1536, vc + 7 * 1536);
        if (c == 7) {
            q1 = q1b;
            q2 = h ? (bf16x4){} : q2b;
#pragma unroll
            for (int r = 0; r < 16; ++r) cc[r] = crow(r, h) <= r32 ? 0.f : -INFINITY;
            asm volatile("" : "+v"(cc), "+v"(q1), "+v"(q2));
        }
        STEP(sb_, sa, cc, q1, q2, kn1, vc + 7 * 1536, vn0);
    }
    DRAIN();

    {
        float l = get_l(o);
        if (__builtin_expect(unsafe(o, l), 0)) {
            bf16x8 q1s = cat8(*(const bf16x4*)(qp + h * 32768), *(const bf16x4*)(qp + h * 32768 + 16384));
            bf16x4 q2s = *(const bf16x4*)(qp + 65536);
            if (h) q2s = (bf16x4){};
            attn_slow(o, kvA, q1s, q2s, 0, 63, 0, qrow, lane);
            l = get_l(o);
        }
        store_o(o, l, ws + WS_O, head, qrow, h);
    }

    {
        o = (f32x16){};
        float mref = rowmax16(sa);
        if (j > 0 || w > 0) {
            f32x16 s = MFMA32(cat8(k1a, k1b), q1, o);
            s = MFMA32K8(k2, q2, s);
            mref = __builtin_fmaxf(mref, rowmax16(s));
        }
        const float nm = -(mref + MBIAS);
#pragma unroll
        for (int r = 0; r < 16; ++r) { sa[r] += nm; cc[r] = nm; }
        {
            pb = (u32x4){};
            pt[0] = pt[1] = pt[2] = pt[3] = 0.f;
            const int kill = j == 0 ? w : -1;
            asm volatile("" : "+v"(cc));
#define KILL(i) do { if (kill == (i)) { _Pragma("unroll") for (int r = 0; r < 16; ++r) cc[r] = -INFINITY; asm volatile("" : "+v"(cc)); } } while (0)
            SETPRIO(2);
            KILL(0); STEP(sa, sb_, cc, q1, q2, kad(w + 6), vad(w + 8), vad(w + 7));
            KILL(1); STEP(sb_, sa, cc, q1, q2, kad(w + 5), vad(w + 7), vad(w + 6));
                KILL(2); STEP(sa, sb_, cc, q1, q2, kad(w + 4), vad(w + 6), vad(w + 5));
            SETPRIO(1);
            KILL(3); STEP(sb_, sa, cc, q1, q2, kad(w + 3), vad(w + 5), vad(w + 4));
                KILL(4); STEP(sa, sb_, cc, q1, q2, kad(w + 2), vad(w + 4), vad(w + 3));
            KILL(5); STEP(sb_, sa, cc, q1, q2, kad(w + 1), vad(w + 3), vad(w + 2));
            SETPRIO(0);
            KILL(6); STEP(sa, sb_, cc, q1, q2, kad(w), vad(w + 2), vad(w + 1));
#pragma unroll
            for (int r = 0; r < 16; ++r) cc[r] = (j > 0 && crow(r, h) > r32) ? nm : -INFINITY;
            STEP(sb_, sa, cc, q1, q2, kad(w), vad(w + 1), vad(w));
            STEP(sa, sb_, cc, q1, q2, kad(w), vad(w), vad(w));
            DRAIN();
        }
        float l = get_l(o);
        if (__builtin_expect(unsafe(o, l), 0)) {
            const int ud = j * 8 + w;
            attn_slow(o, kvB, q1, q2, ud >= 8 ? ud - 8 : 0, ud, 1, qrow, lane);
            l = get_l(o);
        }
        store_o(o, l, ws + WS_O + WS_O_BYTES / 2, head, qrow, h);
    }

}

constexpr int P_STAGE = 36 * 1024, P_LDS = 4 * P_STAGE;

__global__ __launch_bounds__(512, 2) void oproj_kernel(char* __restrict__ ws, __hip_bfloat16* __restrict__ out) {
    extern __shared__ __attribute__((aligned(16))) char lds[];
    const int tid = threadIdx.x, lane = tid & 63;
    const int w = __builtin_amdgcn_readfirstlane(tid >> 6);
    const int bid = blockIdx.x;
    const int mb = (bid & 7) * 4 + (bid >> 6), nb = (bid >> 3) & 7;
    const unsigned lds0 = (unsigned)(uintptr_t)lds;

    const char* src[5];
#pragma unroll
    for (int i = 0; i < 5; ++i) {
        int p = w + 8 * i;
        if (p >= 36) p = w;
        if (p < 16) {
            const int br = p >> 3, mt = (p >> 1) & 3, ks = p & 1;
            src[i] = ws + WS_O + ((size_t)(br * 128 + mb * 4 + mt) * 20 + ks) * 1024 + lane * 16;
        } else {
            const int pb = p - 16, br = pb / 10, nt = (pb % 10) >> 1, ks = pb & 1;
            src[i] = ws + WS_WO + ((size_t)(br * 40 + nb * 5 + nt) * 20 + ks) * 1024 + lane * 16;
        }
    }
    auto piece = [&](int s, int i) {
        if (i < 4 || w < 4) glds16(src[i] + s * 2048, (unsigned)__builtin_amdgcn_readfirstlane(lds0 + (s & 3) * P_STAGE + (w + 8 * i) * 1024));
    };
#pragma unroll
    for (int s = 0; s < 3; ++s)
#pragma unroll
        for (int i = 0; i < 5; ++i) piece(s, i);
    const int br = w >> 2, mh = (w >> 1) & 1, kp = w & 1;
    float g[2];
#pragma unroll
    for (int m = 0; m < 2; ++m) g[m] = ((const float*)(ws + WS_G))[mb * 64 + mh * 32 + m * 16 + (lane & 15)];
    f32x4 acc[2][5];
#pragma unroll
    for (int i = 0; i < 2; ++i)
#pragma unroll
        for (int n = 0; n < 5; ++n) acc[i][n] = (f32x4){};
    const int aoff = ((br * 4 + mh * 2) * 2 + kp) * 1024 + (lane >> 4) * 256 + (lane & 15) * 8;
    const int boff = (16 + br * 10 + kp) * 1024 + lane * 16;

#pragma unroll
    for (int s = 0; s < 10; ++s) {
        if (s <= 7) { if (w < 4) WAIT_BAR(10); else WAIT_BAR(8); }
        else if (s == 8) { if (w < 4) WAIT_BAR(5); else WAIT_BAR(4); }
        else WAIT_BAR(0);
        lds_cptr sb = (lds_cptr)lds + (s & 3) * P_STAGE;
        bf16x8 af[2], bfr[5];
#pragma unroll
        for (int i = 0; i < 2; ++i) af[i] = cat8(lds_b64(sb + aoff + i * 2048), lds_b64(sb + aoff + i * 2048 + 128));
#pragma unroll
        for (int n = 0; n < 5; ++n) bfr[n] = lds_b128(sb + boff + n * 2048);
        if (s + 3 < 10) {
#pragma unroll
            for (int i = 0; i < 5; ++i) piece(s + 3, i);
        }
#pragma unroll
        for (int i = 0; i < 2; ++i)
#pragma unroll
            for (int n = 0; n < 5; ++n) acc[i][n] = MFMA16(bfr[n], af[i], acc[i][n]);
    }

    WAIT_BAR(0);
    f32x4* part = (f32x4*)lds;
#pragma unroll
    for (int m = 0; m < 2; ++m)
#pragma unroll
        for (int n = 0; n < 5; ++n) part[(w * 10 + m * 5 + n) * 64 + lane] = acc[m][n];
    __syncthreads();
    const int qd = br * 2 + kp;
#pragma unroll
    for (int i = 0; i < 3; ++i) {
        const int tl = qd + 4 * i;
        if (tl < 10) {
            const int m = tl >= 5 ? 1 : 0, n = tl - 5 * m;
            const int row = mb * 64 + mh * 32 + m * 16 + (lane & 15);
            const float gg = m ? g[1] : g[0];
            const f32x4 a = part[((mh * 2 + 0) * 10 + tl) * 64 + lane] + part[((mh * 2 + 1) * 10 + tl) * 64 + lane];
            const f32x4 b = part[((4 + mh * 2 + 0) * 10 + tl) * 64 + lane] + part[((4 + mh * 2 + 1) * 10 + tl) * 64 + lane];
            const f32x4 v = gg * a + (1.0f - gg) * b;
            u32x2 pk = {pack2(v[0], v[1]), pack2(v[2], v[3])};
            *(u32x2*)(out + (size_t)row * DM + nb * 80 + n * 16 + (lane >> 4) * 4) = pk;
        }
    }

}

extern "C" void kernel_launch(void* const* d_in, const int* in_sizes, int n_in, void* d_out, int out_size, void* d_ws, size_t ws_size,
                              hipStream_t stream) {
    const __hip_bfloat16* x = (const __hip_bfloat16*)d_in[0];
    const __hip_bfloat16* wA = (const __hip_bfloat16*)d_in[1];
    const __hip_bfloat16* woA = (const __hip_bfloat16*)d_in[2];
    const __hip_bfloat16* wB = (const __hip_bfloat16*)d_in[4];
    const __hip_bfloat16* woB = (const __hip_bfloat16*)d_in[5];
    const __hip_bfloat16* gw = (const __hip_bfloat16*)d_in[6];
    char* ws = (char*)d_ws;
    (void)hipFuncSetAttribute((const void*)qkv_kernel, hipFuncAttributeMaxDynamicSharedMemorySize, G_LDS);
    (void)hipFuncSetAttribute((const void*)attn_kernel, hipFuncAttributeMaxDynamicSharedMemorySize, A_LDS);
    (void)hipFuncSetAttribute((const void*)oproj_kernel, hipFuncAttributeMaxDynamicSharedMemorySize, P_LDS);
    qkv_kernel<<<256, 512, G_LDS, stream>>>(x, wA, wB, woA, woB, gw, ws);
    attn_kernel<<<256, 512, A_LDS, stream>>>(ws);
    oproj_kernel<<<256, 512, P_LDS, stream>>>(ws, (__hip_bfloat16*)d_out);
}
```
